```python
import torch

def _softmax(x, axis=-1):
    return torch.softmax(x, dim=axis)
def _gelu(x):
    return torch.nn.functional.gelu(x, approximate="tanh")
def _silu(x):
    return torch.nn.functional.silu(x)
def _sigmoid(x):
    return torch.sigmoid(x)
def _layernorm(x, axis=-1, eps=1e-5):
    m = x.mean(dim=axis, keepdim=True)
    v = x.var(dim=axis, keepdim=True, unbiased=False)
    return (x - m) / torch.sqrt(v + eps)
def _scatter_add(tgt, idx, src, axis=0):
    return tgt.clone().index_add_(axis, idx, src)
def _segment_sum(src, seg, n):
    out = torch.zeros((n,) + src.shape[1:], dtype=src.dtype)
    return out.index_add_(0, seg, src)
def _pad(x, pw):
    flat = tuple(v for pair in reversed(pw) for v in pair)
    return torch.nn.functional.pad(x, flat)

kernel_name = 'concat_heads__gqa_block_cached__gqa_block_cached__near_ridge'

def setup_inputs():

    _w = (lambda a: (lambda t: t.to(torch.bfloat16) if t.is_floating_point() else t)(torch.as_tensor(a)))
    def _A_setup():
        B, Sq, D = 8, 192, 512
        Hq, Hkv, Sk, Dh = 32, 32, 1024, 128
        assert Hq % Hkv == 0, (Hq, Hkv)
        return {
            "x": _w((lambda *s: torch.randn(*s, dtype=torch.bfloat16) * 0.8)(B, Sq, D)),
            "k_cache": _w((lambda *s: torch.randn(*s, dtype=torch.bfloat16) * 0.8)(B, Hkv, Sk, Dh)),
            "v_cache": _w((lambda *s: torch.randn(*s, dtype=torch.bfloat16) * 0.05)(B, Hkv, Sk, Dh)),
            "wq": _w((lambda *s: torch.randn(*s, dtype=torch.bfloat16) * 0.8)(D, Hq * Dh) * (D ** -0.5)),
            "wo": _w((lambda *s: torch.randn(*s, dtype=torch.bfloat16) * 0.05)(Hq * Dh, D) * ((Hq * Dh) ** -0.5)),
        }
    _A_in = _A_setup()

    def _B_setup():
        B, Sq, D = 8, 192, 512
        Hq, Hkv, Sk, Dh = 32, 32, 1024, 128
        assert Hq % Hkv == 0, (Hq, Hkv)
        return {
            "x": _w((lambda *s: torch.randn(*s, dtype=torch.bfloat16) * 0.8)(B, Sq, D)),
            "k_cache": _w((lambda *s: torch.randn(*s, dtype=torch.bfloat16) * 0.8)(B, Hkv, Sk, Dh)),
            "v_cache": _w((lambda *s: torch.randn(*s, dtype=torch.bfloat16) * 0.05)(B, Hkv, Sk, Dh)),
            "wq": _w((lambda *s: torch.randn(*s, dtype=torch.bfloat16) * 0.8)(D, Hq * Dh) * (D ** -0.5)),
            "wo": _w((lambda *s: torch.randn(*s, dtype=torch.bfloat16) * 0.05)(Hq * Dh, D) * ((Hq * Dh) ** -0.5)),
        }
    _B_in = _B_setup()

    return {
        "A_x": _A_in["x"],
        "A_k_cache": _A_in["k_cache"],
        "A_v_cache": _A_in["v_cache"],
        "A_wq": _A_in["wq"],
        "A_wo": _A_in["wo"],
        "B_x": _B_in["x"],
        "B_k_cache": _B_in["k_cache"],
        "B_v_cache": _B_in["v_cache"],
        "B_wq": _B_in["wq"],
        "B_wo": _B_in["wo"],
    }

def reference(A_x, A_k_cache, A_v_cache, A_wq, A_wo, B_x, B_k_cache, B_v_cache, B_wq, B_wo):
    def _A_ref(x, k_cache, v_cache, wq, wo):
        B, Sq, D = x.shape
        Hq, Dh = 32, 128
        Hkv = k_cache.shape[1]
        rep = Hq // Hkv
        q = (lambda x, *d: x.permute(*d))((x @ wq).reshape(B, Sq, Hq, Dh), 0, 2, 1, 3)
        k = (lambda x, n, axis: torch.repeat_interleave(x, n, dim=axis))(k_cache, rep, axis=1)
        v = (lambda x, n, axis: torch.repeat_interleave(x, n, dim=axis))(v_cache, rep, axis=1)
        scores = torch.einsum("bhqd,bhkd->bhqk", q, k) * (Dh ** -0.5)
        p = _softmax(scores, axis=-1)
        o = torch.einsum("bhqk,bhkd->bhqd", p, v)
        return (lambda x, *d: x.permute(*d))(o, 0, 2, 1, 3).reshape(B, Sq, Hq * Dh) @ wo

    def _B_ref(x, k_cache, v_cache, wq, wo):
        B, Sq, D = x.shape
        Hq, Dh = 32, 128
        Hkv = k_cache.shape[1]
        rep = Hq // Hkv
        q = (lambda x, *d: x.permute(*d))((x @ wq).reshape(B, Sq, Hq, Dh), 0, 2, 1, 3)
        k = (lambda x, n, axis: torch.repeat_interleave(x, n, dim=axis))(k_cache, rep, axis=1)
        v = (lambda x, n, axis: torch.repeat_interleave(x, n, dim=axis))(v_cache, rep, axis=1)
        scores = torch.einsum("bhqd,bhkd->bhqk", q, k) * (Dh ** -0.5)
        p = _softmax(scores, axis=-1)
        o = torch.einsum("bhqk,bhkd->bhqd", p, v)
        return (lambda x, *d: x.permute(*d))(o, 0, 2, 1, 3).reshape(B, Sq, Hq * Dh) @ wo

    a = _A_ref(A_x, A_k_cache, A_v_cache, A_wq, A_wo)
    b = _B_ref(B_x, B_k_cache, B_v_cache, B_wq, B_wo)
    return torch.concatenate([a, b], axis=-1)
```

```cpp
#include <hip/hip_runtime.h>
#include <stdint.h>

typedef __bf16   bf16x8 __attribute__((ext_vector_type(8)));
typedef __bf16   bf16x4 __attribute__((ext_vector_type(4)));
typedef __bf16   bf16x2 __attribute__((ext_vector_type(2)));
typedef float    f32x4  __attribute__((ext_vector_type(4)));
typedef float    f32x2  __attribute__((ext_vector_type(2)));
typedef unsigned u32x4  __attribute__((ext_vector_type(4)));
typedef unsigned u32x2  __attribute__((ext_vector_type(2)));
#define LAS __attribute__((address_space(3)))
#define DI __device__ __forceinline__

constexpr int NB = 8, SQ = 192, DM = 512, NH = 32, SK = 1024, DH = 128;
constexpr int NSLOT = 5, SLOT = 16384, LDS1 = NSLOT * SLOT;
constexpr int NITEM = 8 + SK / 32;
constexpr float QSCALE = 0.08838834764831845f * 1.4426950408889634f;
constexpr float SUMTHR = 1048576.f;

DI unsigned rfl(unsigned x) { return __builtin_amdgcn_readfirstlane(x); }
DI unsigned cvtpk(float lo, float hi) { f32x2 v = {lo, hi}; bf16x2 b = __builtin_convertvector(v, bf16x2); return __builtin_bit_cast(unsigned, b); }
DI f32x4 mfma16(bf16x8 a, bf16x8 b, f32x4 c) { return __builtin_amdgcn_mfma_f32_16x16x32_bf16(a, b, c, 0, 0, 0); }
DI bf16x4 trread(unsigned addr) { return __builtin_amdgcn_ds_read_tr16_b64_v4bf16((LAS bf16x4*)addr); }
DI bf16x8 cat(bf16x4 a, bf16x4 b) { return __builtin_shufflevector(a, b, 0, 1, 2, 3, 4, 5, 6, 7); }

#define DMA4(m0v, v0, v1, v2, v3, srd, soff, POL) \
  asm volatile("s_mov_b32 m0, %0\n\ts_nop 0\n\t" \
               "buffer_load_dwordx4 %1, %5, %6 offen " POL " lds\n\t" \
               "buffer_load_dwordx4 %2, %5, %6 offen offset:1024 " POL " lds\n\t" \
               "buffer_load_dwordx4 %3, %5, %6 offen offset:2048 " POL " lds\n\t" \
               "buffer_load_dwordx4 %4, %5, %6 offen offset:3072 " POL " lds" \
               :: "s"(m0v), "v"(v0), "v"(v1), "v"(v2), "v"(v3), "s"(srd), "s"(soff) : "memory")

#define KVPOL "nt"

DI float xor16(float v) { auto r = __builtin_amdgcn_permlane16_swap(__float_as_uint(v), __float_as_uint(v), false, false); return 0.f; }

DI float red4max(float v) {
  auto a = __builtin_amdgcn_permlane16_swap(__float_as_uint(v), __float_as_uint(v), false, false);
  v = fmaxf(__uint_as_float(a[0]), __uint_as_float(a[1]));
  auto b = __builtin_amdgcn_permlane32_swap(__float_as_uint(v), __float_as_uint(v), false, false);
  return fmaxf(__uint_as_float(b[0]), __uint_as_float(b[1]));
}
DI float red4sum(float v) {
  auto a = __builtin_amdgcn_permlane16_swap(__float_as_uint(v), __float_as_uint(v), false, false);
  v = __uint_as_float(a[0]) + __uint_as_float(a[1]);
  auto b = __builtin_amdgcn_permlane32_swap(__float_as_uint(v), __float_as_uint(v), false, false);
  return __uint_as_float(b[0]) + __uint_as_float(b[1]);
}

__global__ __launch_bounds__(256, 2)
void attn_kernel(const uint16_t* __restrict__ xA, const uint16_t* __restrict__ kA, const uint16_t* __restrict__ vA, const uint16_t* __restrict__ wqA,
                 const uint16_t* __restrict__ xB, const uint16_t* __restrict__ kB, const uint16_t* __restrict__ vB, const uint16_t* __restrict__ wqB,
                 uint16_t* __restrict__ Ows) {
  extern __shared__ __attribute__((aligned(16))) char lds[];
  const unsigned ldsb = (unsigned)(uintptr_t)(LAS char*)lds;
  const unsigned tid = threadIdx.x, lane = tid & 63u, w = rfl(tid >> 6);
  const unsigned g = lane >> 4, i16 = lane & 15u;
  const unsigned bid = blockIdx.x, xcd = bid & 7u, idx = bid >> 3;
  const unsigned br = xcd >> 2, h = (xcd & 3u) * 8u + (idx & 7u), b = idx >> 3;
  const uint16_t* x  = br ? xB : xA;
  const uint16_t* kc = br ? kB : kA;
  const uint16_t* vc = br ? vB : vA;
  const uint16_t* wq = br ? wqB : wqA;
  const size_t kvoff = (size_t)(b * NH + h) * SK * DH;
  const uint16_t* kvp = (w < 2 ? kc : vc) + kvoff;
  __amdgpu_buffer_rsrc_t srdKV = __builtin_amdgcn_make_buffer_rsrc((void*)kvp, (short)0, SK * DH * 2, 0x00020000);
  __amdgpu_buffer_rsrc_t srdW  = __builtin_amdgcn_make_buffer_rsrc((void*)(wq + h * DH), (short)0, DM * NH * DH * 2 - h * DH * 2, 0x00020000);

  const unsigned dr = (lane >> 2) & 3u, cp = lane & 3u;
  unsigned kvo[4], wqo[4];
#pragma unroll
  for (int a = 0; a < 4; ++a) {
    const unsigned f = (w < 2) ? (((a >> 1) & 1) << 1) : (a & 1);
    kvo[a] = dr * 256u + g * 64u + ((cp ^ f) << 4);
    wqo[a] = dr * 8192u + g * 64u + ((cp ^ (a & 1)) << 4) + a * (65536u - 1024u);
  }
  const unsigned m0w = ldsb + w * 4096u;
  const unsigned wq_s0 = ((w >> 1) * 32u + (w & 1u) * 4u) * 8192u;
  const unsigned kv_s0 = (w & 1u) * 4096u;
  auto issue = [&](int item, unsigned slot) {
    if (item < 8) { DMA4(m0w + slot * SLOT, wqo[0], wqo[1], wqo[2], wqo[3], srdW, wq_s0 + (unsigned)item * 524288u, ""); }
    else          { DMA4(m0w + slot * SLOT, kvo[0], kvo[1], kvo[2], kvo[3], srdKV, kv_s0 + (unsigned)(item - 8) * 8192u, KVPOL); }
  };
  const unsigned qp = (lane >> 2) & 3u, p = lane & 3u;
  const unsigned tra0 = ldsb + g * 1024u + qp * 64u + ((p ^ (g & 1u)) << 4) + ((p & 1u) << 3);
  const unsigned tra1 = tra0 ^ 8u;
  const unsigned kra = ldsb + (i16 >> 2) * 1024u + (i16 & 3u) * 64u + ((g ^ (((i16 >> 3) & 1u) << 1)) << 4);

  issue(0, 0); issue(1, 1); issue(2, 2); issue(3, 3);

  f32x4 acc[8][3];
#pragma unroll
  for (int d = 0; d < 8; ++d)
#pragma unroll
    for (int qb = 0; qb < 3; ++qb) acc[d][qb] = f32x4{0.f, 0.f, 0.f, 0.f};
  const uint16_t* xrow = x + (size_t)(b * SQ + w * 48u + i16) * DM + g * 8u;
  bf16x8 xf[2][2][3];
#pragma unroll
  for (int t = 0; t < 2; ++t)
#pragma unroll
    for (int qb = 0; qb < 3; ++qb) xf[0][t][qb] = *(const bf16x8*)(xrow + qb * 16 * DM + t * 32);
  unsigned slot = 0, slot_in = 4;
#pragma unroll
  for (int n = 0; n < 8; ++n) {
    if (n < 7) {
#pragma unroll
      for (int t = 0; t < 2; ++t)
#pragma unroll
        for (int qb = 0; qb < 3; ++qb) xf[(n + 1) & 1][t][qb] = *(const bf16x8*)(xrow + qb * 16 * DM + (n + 1) * 64 + t * 32);
    }
    asm volatile("s_waitcnt vmcnt(12)" ::: "memory");
    __builtin_amdgcn_s_barrier();
    issue(n + 4, slot_in);
    const unsigned sb = slot * SLOT;
#pragma unroll
    for (int t = 0; t < 2; ++t)
#pragma unroll
      for (int s = 0; s < 4; ++s)
#pragma unroll
        for (int z = 0; z < 2; ++z) {
          const unsigned a = (z ? tra1 : tra0) + sb + t * 8192 + s * 256;
          const bf16x8 A = cat(trread(a), trread(a + 4096));
#pragma unroll
          for (int qb = 0; qb < 3; ++qb) acc[2 * s + z][qb] = mfma16(A, xf[n & 1][t][qb], acc[2 * s + z][qb]);
        }
    slot_in = slot; slot = (slot == NSLOT - 1) ? 0 : slot + 1;
    asm volatile("s_waitcnt lgkmcnt(0)" ::: "memory");
  }
  bf16x8 qf[4][3];
  {
    const bool odd = g & 1u;
#pragma unroll
    for (int s = 0; s < 4; ++s)
#pragma unroll
      for (int qb = 0; qb < 3; ++qb) {
        const f32x4 X = acc[2 * s][qb] * QSCALE, Y = acc[2 * s + 1][qb] * QSCALE;
        const unsigned x0 = cvtpk(X[0], X[1]), x1 = cvtpk(X[2], X[3]), y0 = cvtpk(Y[0], Y[1]), y1 = cvtpk(Y[2], Y[3]);
        u32x4 r = {odd ? y0 : x0, odd ? y1 : x1, odd ? x0 : y0, odd ? x1 : y1};
        qf[s][qb] = __builtin_bit_cast(bf16x8, r);
      }
  }
#pragma unroll
  for (int d = 0; d < 8; ++d)
#pragma unroll
    for (int qb = 0; qb < 3; ++qb) acc[d][qb] = f32x4{0.f, 0.f, 0.f, 0.f};
  float negm[3] = {0.f, 0.f, 0.f}, lsum[3] = {0.f, 0.f, 0.f};
  for (int t = 0; t < SK / 32; ++t) {
    if (t < SK / 32 - 3)       asm volatile("s_waitcnt vmcnt(12)" ::: "memory");
    else                       asm volatile("s_waitcnt vmcnt(0)" ::: "memory");
    __builtin_amdgcn_s_barrier();
    if (t + 12 < NITEM) issue(t + 12, slot_in);
    const unsigned sb = slot * SLOT;
    f32x4 S[2][3];
#pragma unroll
    for (int kb = 0; kb < 2; ++kb) {
#pragma unroll
      for (int qb = 0; qb < 3; ++qb) S[kb][qb] = f32x4{negm[qb], negm[qb], negm[qb], negm[qb]};
#pragma unroll
      for (int s = 0; s < 4; ++s) {
        const bf16x8 Kf = *(const LAS bf16x8*)(kra + sb + kb * 4096 + s * 256);
#pragma unroll
        for (int qb = 0; qb < 3; ++qb) S[kb][qb] = mfma16(Kf, qf[s][qb], S[kb][qb]);
      }
    }
    bf16x8 P[3]; float ts[3];
    auto sm = [&]() {
#pragma unroll
      for (int qb = 0; qb < 3; ++qb) {
        float e[8];
#pragma unroll
        for (int j = 0; j < 8; ++j) e[j] = __builtin_amdgcn_exp2f(S[j >> 2][qb][j & 3]);
        ts[qb] = ((e[0] + e[1]) + (e[2] + e[3])) + ((e[4] + e[5]) + (e[6] + e[7]));
        u32x4 r = {cvtpk(e[0], e[1]), cvtpk(e[2], e[3]), cvtpk(e[4], e[5]), cvtpk(e[6], e[7])};
        P[qb] = __builtin_bit_cast(bf16x8, r);
      }
    };
    sm();
    const bool bad = fmaxf(fmaxf(ts[0], ts[1]), ts[2]) > SUMTHR;
    if (__builtin_expect(t == 0 || __any(bad), 0)) {
#pragma unroll
      for (int qb = 0; qb < 3; ++qb) {
        float m = fmaxf(fmaxf(fmaxf(S[0][qb][0], S[0][qb][1]), fmaxf(S[0][qb][2], S[0][qb][3])),
                        fmaxf(fmaxf(S[1][qb][0], S[1][qb][1]), fmaxf(S[1][qb][2], S[1][qb][3])));
        m = red4max(m);
        if (t != 0) m = fmaxf(m, 0.f);
        negm[qb] -= m;
        S[0][qb] -= m; S[1][qb] -= m;
        if (t != 0) {
          const float f = __builtin_amdgcn_exp2f(-m);
          lsum[qb] *= f;
#pragma unroll
          for (int d = 0; d < 8; ++d) acc[d][qb] *= f;
        }
      }
      sm();
    }
#pragma unroll
    for (int qb = 0; qb < 3; ++qb) lsum[qb] += ts[qb];
#pragma unroll
    for (int s = 0; s < 4; ++s)
#pragma unroll
      for (int z = 0; z < 2; ++z) {
        const unsigned a = (z ? tra1 : tra0) + sb + 8192 + s * 256;
        const bf16x8 A = cat(trread(a), trread(a + 4096));
#pragma unroll
        for (int qb = 0; qb < 3; ++qb) acc[2 * s + z][qb] = mfma16(A, P[qb], acc[2 * s + z][qb]);
      }
    slot_in = slot; slot = (slot == NSLOT - 1) ? 0 : slot + 1;
    asm volatile("s_waitcnt lgkmcnt(0)" ::: "memory");
  }
#pragma unroll
  for (int qb = 0; qb < 3; ++qb) {
    const float il = 1.0f / red4sum(lsum[qb]);
    uint16_t* op = Ows + ((((size_t)br * 96u + b * 12u + w * 3u + qb) * 128u + h * 4u) * 64u + lane) * 8u;
#pragma unroll
    for (int s = 0; s < 4; ++s) {
      const f32x4 X = acc[2 * s][qb] * il, Y = acc[2 * s + 1][qb] * il;
      u32x4 r = {cvtpk(X[0], X[1]), cvtpk(X[2], X[3]), cvtpk(Y[0], Y[1]), cvtpk(Y[2], Y[3])};
      *(u32x4*)(op + s * 512) = r;
    }
  }
}

constexpr int ODEPTH = 4;
constexpr int LDS2 = 4 * 24576;
__global__ __launch_bounds__(256, 2)
void oproj_kernel(const uint16_t* __restrict__ Ows, const uint16_t* __restrict__ woA, const uint16_t* __restrict__ woB, uint16_t* __restrict__ out) {
  extern __shared__ __attribute__((aligned(16))) char lds[];
  const unsigned ldsb = (unsigned)(uintptr_t)(LAS char*)lds;
  const unsigned tid = threadIdx.x, lane = tid & 63u, w = rfl(tid >> 6);
  const unsigned g = lane >> 4;
  const unsigned bid = blockIdx.x, xcd = bid & 7u, idx = bid >> 3;
  const unsigned br = xcd >> 2, mt = (xcd & 3u) * 4u + (idx >> 3), nt = idx & 7u;
  const uint16_t* wo = br ? woB : woA;
  __amdgpu_buffer_rsrc_t srdW = __builtin_amdgcn_make_buffer_rsrc((void*)(wo + nt * 64u), (short)0, NH * DH * DM * 2 - nt * 128u, 0x00020000);
  unsigned wvo[4];
#pragma unroll
  for (int pc = 0; pc < 4; ++pc) {
    const unsigned a = 2u * pc + (lane >> 5), jh = a >> 2, gg = a & 3u;
    const unsigned row = 8u * gg + 4u * ((gg & 1u) ^ jh) + ((lane >> 2) & 3u);
    wvo[pc] = row * 1024u + ((lane >> 4) & 1u) * 64u + (((lane & 3u) ^ (gg & 1u)) << 4) + 4096u - 1024u * pc;
  }
  constexpr int RING = ODEPTH + 1;
  const unsigned ring0 = ldsb + w * (RING * 4096u);
  const unsigned qp = (lane >> 2) & 3u, p = lane & 3u;
  const unsigned tra0 = ring0 + g * 512u + qp * 64u + ((p ^ (g & 1u)) << 4) + ((p & 1u) << 3), tra1 = tra0 ^ 8u;
  const uint16_t* Op = Ows + (((size_t)br * 96u + mt * 6u) * 128u + w * 32u) * 512u + lane * 8u;
  const unsigned ws0 = w * 32u * 32u * 1024u;
  f32x4 acc[4][6];
#pragma unroll
  for (int d = 0; d < 4; ++d)
#pragma unroll
    for (int qb = 0; qb < 6; ++qb) acc[d][qb] = f32x4{0.f, 0.f, 0.f, 0.f};
  __amdgpu_buffer_rsrc_t srdWb = __builtin_amdgcn_make_buffer_rsrc((void*)((const char*)(wo + nt * 64u) - 4096), (short)0, NH * DH * DM * 2 - nt * 128u + 4096u, 0x00020000);
  bf16x8 of[RING][6];
  auto issue = [&](int ks, int slot) {
    DMA4(ring0 + slot * 4096u, wvo[0], wvo[1], wvo[2], wvo[3], srdWb, ws0 + (unsigned)ks * 32768u, "");
#pragma unroll
    for (int qb = 0; qb < 6; ++qb)
      asm volatile("global_load_dwordx4 %0, %1, off" : "=v"(of[slot][qb]) : "v"(Op + (size_t)qb * 128 * 512 + ks * 512) : "memory");
  };
#pragma unroll
  for (int i = 0; i < ODEPTH; ++i) issue(i, i);
#pragma unroll
  for (int ks = 0; ks < 32; ++ks) {
    const int u = ks % RING;
    if (ks + ODEPTH < 32) {
      issue(ks + ODEPTH, (u + ODEPTH) % RING);
      asm volatile("s_waitcnt vmcnt(40)" : "+v"(of[u][0]), "+v"(of[u][1]), "+v"(of[u][2]), "+v"(of[u][3]), "+v"(of[u][4]), "+v"(of[u][5]) :: "memory");
    } else if (ks == 28) {
      asm volatile("s_waitcnt vmcnt(30)" : "+v"(of[u][0]), "+v"(of[u][1]), "+v"(of[u][2]), "+v"(of[u][3]), "+v"(of[u][4]), "+v"(of[u][5]) :: "memory");
    } else if (ks == 29) {
      asm volatile("s_waitcnt vmcnt(20)" : "+v"(of[u][0]), "+v"(of[u][1]), "+v"(of[u][2]), "+v"(of[u][3]), "+v"(of[u][4]), "+v"(of[u][5]) :: "memory");
    } else if (ks == 30) {
      asm volatile("s_waitcnt vmcnt(10)" : "+v"(of[u][0]), "+v"(of[u][1]), "+v"(of[u][2]), "+v"(of[u][3]), "+v"(of[u][4]), "+v"(of[u][5]) :: "memory");
    } else {
      asm volatile("s_waitcnt vmcnt(0)" : "+v"(of[u][0]), "+v"(of[u][1]), "+v"(of[u][2]), "+v"(of[u][3]), "+v"(of[u][4]), "+v"(of[u][5]) :: "memory");
    }
#pragma unroll
    for (int s = 0; s < 2; ++s)
#pragma unroll
      for (int z = 0; z < 2; ++z) {
        const unsigned a = (z ? tra1 : tra0) + u * 4096 + s * 256;
        const bf16x8 A = cat(trread(a), trread(a + 2048));
#pragma unroll
        for (int qb = 0; qb < 6; ++qb) acc[2 * s + z][qb] = mfma16(A, of[u][qb], acc[2 * s + z][qb]);
      }
    asm volatile("s_waitcnt lgkmcnt(0)" ::: "memory");
  }
  __syncthreads();
  LAS f32x4* red = (LAS f32x4*)ldsb;
#pragma unroll
  for (int qb = 0; qb < 6; ++qb)
#pragma unroll
    for (int d = 0; d < 4; ++d) red[((w * 6 + qb) * 4 + d) * 64 + lane] = acc[d][qb];
  __syncthreads();
#pragma unroll
  for (int it = 0; it < 3; ++it) {
    const unsigned e = it * 4u + w, qb = e >> 1, s = e & 1u;
    f32x4 X = {0.f, 0.f, 0.f, 0.f}, Y = X;
#pragma unroll
    for (int ww = 0; ww < 4; ++ww) { X += red[((ww * 6 + qb) * 4 + 2 * s) * 64 + lane]; Y += red[((ww * 6 + qb) * 4 + 2 * s + 1) * 64 + lane]; }
    const unsigned x0 = cvtpk(X[0], X[1]), x1 = cvtpk(X[2], X[3]), y0 = cvtpk(Y[0], Y[1]), y1 = cvtpk(Y[2], Y[3]);
    const bool odd = g & 1u;
    u32x4 r = {odd ? y0 : x0, odd ? y1 : x1, odd ? x0 : y0, odd ? x1 : y1};
    *(u32x4*)(out + (size_t)(mt * 96u + qb * 16u + (lane & 15u)) * 1024u + br * 512u + nt * 64u + s * 32u + g * 8u) = r;
  }
}

extern "C" void kernel_launch(void* const* d_in, const int* in_sizes, int n_in, void* d_out, int out_size, void* d_ws, size_t ws_size, hipStream_t stream) {
  (void)hipFuncSetAttribute((const void*)attn_kernel, hipFuncAttributeMaxDynamicSharedMemorySize, LDS1);
  (void)hipFuncSetAttribute((const void*)oproj_kernel, hipFuncAttributeMaxDynamicSharedMemorySize, LDS2);
  uint16_t* Ows = (uint16_t*)d_ws;
  attn_kernel<<<512, 256, LDS1, stream>>>((const uint16_t*)d_in[0], (const uint16_t*)d_in[1], (const uint16_t*)d_in[2], (const uint16_t*)d_in[3],
                                          (const uint16_t*)d_in[5], (const uint16_t*)d_in[6], (const uint16_t*)d_in[7], (const uint16_t*)d_in[8], Ows);
  oproj_kernel<<<256, 256, LDS2, stream>>>(Ows, (const uint16_t*)d_in[4], (const uint16_t*)d_in[9], (uint16_t*)d_out);
}
```

```cpp
#include <hip/hip_runtime.h>
#include <stdint.h>

typedef unsigned u32x4 __attribute__((ext_vector_type(4)));
typedef unsigned u32x8 __attribute__((ext_vector_type(8)));
#define LAS __attribute__((address_space(3)))
constexpr int SQ = 192, DM = 512, NH = 32, SK = 1024, DH = 128;
constexpr int LDS1 = 5 * 16384;
constexpr size_t WS_FLAGS = 32u << 20;
__device__ __forceinline__ u32x4 mksrd(const void* p, unsigned bytes) {
  const uint64_t a = (uint64_t)(uintptr_t)p;
  u32x4 r = {(unsigned)a, (unsigned)(a >> 32) & 0xffffu, bytes, 0x00020000u};
  return r;
}

__global__ __launch_bounds__(256, 2)
void fused_kernel(const uint16_t* __restrict__ xA, const uint16_t* __restrict__ kA, const uint16_t* __restrict__ vA, const uint16_t* __restrict__ wqA, const uint16_t* __restrict__ woA,
                  const uint16_t* __restrict__ xB, const uint16_t* __restrict__ kB, const uint16_t* __restrict__ vB, const uint16_t* __restrict__ wqB, const uint16_t* __restrict__ woB,
                  uint16_t* __restrict__ out, char* __restrict__ ws) {
  extern __shared__ __attribute__((aligned(16))) char lds[];
  const unsigned ldsb = (unsigned)(uintptr_t)(LAS char*)lds;
  const unsigned tid = threadIdx.x, lane = tid & 63u, w = __builtin_amdgcn_readfirstlane(tid >> 6);
  const unsigned g = lane >> 4, i16 = lane & 15u;
  const unsigned bid = blockIdx.x, xcd = bid & 7u, idx = bid >> 3;
  const unsigned br = xcd >> 2, hl = idx & 3u, h = (xcd & 3u) * 8u + (idx >> 5) * 4u + hl, b = (idx >> 2) & 7u;
  const uint16_t* x  = br ? xB : xA;
  const uint16_t* kc = br ? kB : kA;
  const uint16_t* vc = br ? vB : vA;
  const uint16_t* wq = br ? wqB : wqA;
  const uint16_t* wo = br ? woB : woA;
  const u32x4 srdKV = mksrd((w < 2 ? kc : vc) + (size_t)(b * NH + h) * SK * DH, (SK * DH * 2));
  const u32x4 srdW  = mksrd(wq + h * DH, (DM * NH * DH * 2 - h * DH * 2));
  u32x4 srdX  = mksrd(x + (size_t)(b * SQ + w * 48u) * DM, (48 * DM * 2));
  const unsigned hg = xcd & 3u;
  const u32x4 srdO  = mksrd(ws + ((size_t)(br * 8u + b) * 128u + h * 4u) * 12288u + w * 3072u, (3 * 12288 + 3072));
  u32x4 srdOL = mksrd(ws + ((size_t)(br * 8u + b) * 128u + hg * 32u) * 12288u + w * 3072u, (31 * 12288 + 3072));
  const u32x4 srdWO  = mksrd(wo + (size_t)hg * 1024u * DM + hl * 128u, (1023 * 1024 + 256));
  const u32x4 srdOUT = mksrd(out + (size_t)(b * 192u + (hg >> 1) * 96u) * 1024u + br * 512u + hl * 128u + (hg & 1u) * 64u, 95 * 2048 + 128);
  const u32x4 srdFL = mksrd(ws + WS_FLAGS, 32768);
  const uint64_t kbase = (uint64_t)(uintptr_t)(kc + (size_t)(b * NH + h) * SK * DH) & 0xffffffffffffull;
  const unsigned b3 = b % 3u, e1 = 1u + b3, e2 = 1u + (b3 + 1u) % 3u, e3 = 1u + (b3 + 2u) % 3u, r4 = hl + b;
  const unsigned horder = hl | ((hl ^ e1) << 2) | ((hl ^ e2) << 4) | ((hl ^ e3) << 6) | ((r4 & 3u) << 8) | (((r4 + 1u) & 3u) << 10) | (((r4 + 2u) & 3u) << 12) | (((r4 + 3u) & 3u) << 14);
  const unsigned dbgoff = 0;
  const unsigned dr = (lane >> 2) & 3u, cp = lane & 3u;
  const unsigned rot = (((hl + b) & 3u) << 1) | (b >> 2);
  const unsigned kvo0 = dr * 256u + g * 64u + (cp << 4), kvo1 = kvo0 ^ (32u >> (w >> 1));
  unsigned wqo = dr * 8192u + g * 64u + (cp << 4) + ((w >> 1) * 8u + (w & 1u) * 4u) * 8192u;
  unsigned tra0 = ldsb + g * 1024u + dr * 64u + ((cp ^ (g & 1u)) << 4) + ((cp & 1u) << 3);
  unsigned kra = ldsb + 3 * 16384u + (i16 >> 2) * 1024u + (i16 & 3u) * 64u + ((g ^ (((i16 >> 2) & 1u) << 1)) << 4);
  unsigned xo = dr * 4096u + g * 64u + ((cp ^ ((rot & 1u) << 1)) << 4);
  const unsigned ooff = lane * 16u;
  const unsigned role = (bid >> 8) + ((bid * 4u + w) == 1024u ? 1u : 0u);
  u32x8 sc = {ldsb + w * 4096u, 0u - (w >> 1), (w & 1u) * 4096u, rot | (((hl + 1u * b) & 3u) << 8) | (hg << 16) | (((br * 8u + b) * 4u + hl) << 20), role,
              ((br * 8u + b) * 32u + h) * 8u + w * 0x10000000u, ((br * 8u + b) * 32u + 8u * hg) * 8u, ldsb + w * 20480u};

  asm volatile(
    "s_nop 1\n"
    "v_xor_b32 v251, 16, v250\n"
    "v_add_u32 v252, 522240, v250\n"
    "v_add_u32 v253, 783360, v251\n"
    "v_add_u32 v251, 261120, v251\n"
    "s_cmp_eq_u32 s72, 0\n"
    "s_cbranch_scc1 Lnos0%=\n"
    "s_sleep 30\n"
    "Lnos0%=:\n"
    "s_add_u32 s33, s69, 0\n"
    "s_and_b32 s34, s71, 6\n"
    "s_add_u32 s12, s33, s34\n"
    "s_and_b32 s12, s12, 6\n"
    "s_lshl_b32 s12, s12, 19\n"
    "s_xor_b32 s34, s33, s71\n"
    "s_and_b32 s34, s34, 1\n"
    "s_lshl_b32 s34, s34, 17\n"
    "s_add_u32 s12, s12, s34\n"
    "s_cmp_eq_u32 s33, -1\n"
    "s_cselect_b32 s12, 0x40000000, s12\n"
    "s_add_u32 m0, s68, 0\n"
    "s_nop 0\n"
    "buffer_load_dwordx4 v250, s[40:43], s12 offen offset:0  lds\n"
    "buffer_load_dwordx4 v251, s[40:43], s12 offen offset:1024  lds\n"
    "buffer_load_dwordx4 v252, s[40:43], s12 offen offset:2048  lds\n"
    "buffer_load_dwordx4 v253, s[40:43], s12 offen offset:3072  lds\n"
    "s_lshr_b32 s21, s71, 1\n"
    "s_add_u32 s21, s21, 0\n"
    "s_and_b32 s21, s21, 3\n"
    "s_lshl_b32 s21, s21, 8\n"
    "s_add_u32 s22, s21, 16384\n"
    "s_add_u32 s23, s21, 32768\n"
    "buffer_load_dwordx4 v[96:99], v254, s[44:47], s21 offen offset:0 \n"
    "buffer_load_dwordx4 v[100:103], v254, s[44:47], s21 offen offset:1024 \n"
    "buffer_load_dwordx4 v[104:107], v254, s[44:47], s21 offen offset:2048 \n"
    "buffer_load_dwordx4 v[108:111], v254, s[44:47], s21 offen offset:3072 \n"
    "buffer_load_dwordx4 v[112:115], v254, s[44:47], s22 offen offset:0 \n"
    "buffer_load_dwordx4 v[116:119], v254, s[44:47], s22 offen offset:1024 \n"
    "buffer_load_dwordx4 v[120:123], v254, s[44:47], s22 offen offset:2048 \n"
    "buffer_load_dwordx4 v[124:127], v254, s[44:47], s22 offen offset:3072 \n"
    "buffer_load_dwordx4 v[128:131], v254, s[44:47], s23 offen offset:0 \n"
    "buffer_load_dwordx4 v[132:135], v254, s[44:47], s23 offen offset:1024 \n"
    "buffer_load_dwordx4 v[136:139], v254, s[44:47], s23 offen offset:2048 \n"
    "buffer_load_dwordx4 v[140:143], v254, s[44:47], s23 offen offset:3072 \n"
    "s_add_u32 s33, s69, 1\n"
    "s_and_b32 s34, s71, 6\n"
    "s_add_u32 s13, s33, s34\n"
    "s_and_b32 s13, s13, 6\n"
    "s_lshl_b32 s13, s13, 19\n"
    "s_xor_b32 s34, s33, s71\n"
    "s_and_b32 s34, s34, 1\n"
    "s_lshl_b32 s34, s34, 17\n"
    "s_add_u32 s13, s13, s34\n"
    "s_add_u32 m0, s68, 16384\n"
    "s_nop 0\n"
    "buffer_load_dwordx4 v250, s[40:43], s13 offen offset:0  lds\n"
    "buffer_load_dwordx4 v251, s[40:43], s13 offen offset:1024  lds\n"
    "buffer_load_dwordx4 v252, s[40:43], s13 offen offset:2048  lds\n"
    "buffer_load_dwordx4 v253, s[40:43], s13 offen offset:3072  lds\n"
    "s_buffer_load_dwordx2 s[88:89], s[64:67], 0x1000\n"
    "s_add_u32 s33, s69, 2\n"
    "s_and_b32 s34, s71, 6\n"
    "s_add_u32 s14, s33, s34\n"
    "s_and_b32 s14, s14, 6\n"
    "s_lshl_b32 s14, s14, 19\n"
    "s_xor_b32 s34, s33, s71\n"
    "s_and_b32 s34, s34, 1\n"
    "s_lshl_b32 s34, s34, 17\n"
    "s_add_u32 s14, s14, s34\n"
    "s_add_u32 s33, s69, 3\n"
    "s_and_b32 s34, s71, 6\n"
    "s_add_u32 s15, s33, s34\n"
    "s_and_b32 s15, s15, 6\n"
    "s_lshl_b32 s15, s15, 19\n"
    "s_xor_b32 s34, s33, s71\n"
    "s_and_b32 s34, s34, 1\n"
    "s_lshl_b32 s34, s34, 17\n"
    "s_add_u32 s15, s15, s34\n"
    "s_add_u32 s33, s69, 4\n"
    "s_and_b32 s34, s71, 6\n"
    "s_add_u32 s16, s33, s34\n"
    "s_and_b32 s16, s16, 6\n"
    "s_lshl_b32 s16, s16, 19\n"
    "s_xor_b32 s34, s33, s71\n"
    "s_and_b32 s34, s34, 1\n"
    "s_lshl_b32 s34, s34, 17\n"
    "s_add_u32 s16, s16, s34\n"
    "s_add_u32 s33, s69, 5\n"
    "s_and_b32 s34, s71, 6\n"
    "s_add_u32 s17, s33, s34\n"
    "s_and_b32 s17, s17, 6\n"
    "s_lshl_b32 s17, s17, 19\n"
    "s_xor_b32 s34, s33, s71\n"
    "s_and_b32 s34, s34, 1\n"
    "s_lshl_b32 s34, s34, 17\n"
    "s_add_u32 s17, s17, s34\n"
    "s_add_u32 s33, s69, 6\n"
    "s_and_b32 s34, s71, 6\n"
    "s_add_u32 s18, s33, s34\n"
    "s_and_b32 s18, s18, 6\n"
    "s_lshl_b32 s18, s18, 19\n"
    "s_xor_b32 s34, s33, s71\n"
    "s_and_b32 s34, s34, 1\n"
    "s_lshl_b32 s34, s34, 17\n"
    "s_add_u32 s18, s18, s34\n"
    "s_add_u32 s33, s69, 7\n"
    "s_and_b32 s34, s71, 6\n"
    "s_add_u32 s19, s33, s34\n"
    "s_and_b32 s19, s19, 6\n"
    "s_lshl_b32 s19, s19, 19\n"
    "s_xor_b32 s34, s33, s71\n"
    "s_and_b32 s34, s34, 1\n"
    "s_lshl_b32 s34, s34, 17\n"
    "s_add_u32 s19, s19, s34\n"
    "s_add_u32 s33, s69, 8\n"
    "s_and_b32 s34, s71, 6\n"
    "s_add_u32 s20, s33, s34\n"
    "s_and_b32 s20, s20, 6\n"
    "s_lshl_b32 s20, s20, 19\n"
    "s_xor_b32 s34, s33, s71\n"
    "s_and_b32 s34, s34, 1\n"
    "s_lshl_b32 s34, s34, 17\n"
    "s_add_u32 s20, s20, s34\n"
    "s_lshr_b32 s24, s71, 1\n"
    "s_add_u32 s24, s24, 1\n"
    "s_and_b32 s24, s24, 3\n"
    "s_lshl_b32 s24, s24, 8\n"
    "s_add_u32 s25, s24, 16384\n"
    "s_add_u32 s26, s24, 32768\n"
    "s_lshr_b32 s27, s71, 1\n"
    "s_add_u32 s27, s27, 2\n"
    "s_and_b32 s27, s27, 3\n"
    "s_lshl_b32 s27, s27, 8\n"
    "s_add_u32 s28, s27, 16384\n"
    "s_add_u32 s29, s27, 32768\n"
    "s_lshr_b32 s30, s71, 1\n"
    "s_add_u32 s30, s30, 3\n"
    "s_and_b32 s30, s30, 3\n"
    "s_lshl_b32 s30, s30, 8\n"
    "s_add_u32 s31, s30, 16384\n"
    "s_add_u32 s32, s30, 32768\n"
    "s_mov_b32 s4, 0x55555555\n"
    "s_mov_b32 s5, 0x55555555\n"
    "s_mov_b32 s6, 0xaaaaaaaa\n"
    "s_mov_b32 s7, 0xaaaaaaaa\n"
    "s_mov_b32 s8, 0x33333333\n"
    "s_mov_b32 s9, 0x33333333\n"
    "s_mov_b32 s10, 0xcccccccc\n"
    "s_mov_b32 s11, 0xcccccccc\n"
    "s_mov_b32 s81, 16384\n"
    "s_mov_b32 s82, 0xffff0000\n"
    "s_mov_b32 s84, 0xffff0000\n"
    "s_mov_b32 s85, 0xffff0000\n"
    "s_mov_b32 s86, 0x3e0293ee\n"
    "s_mov_b32 s87, 0x3e0293ee\n"
    "s_mov_b32 s79, 0x49800000\n"
    "v_xor_b32 v244, 8, v243\n"
    "s_waitcnt vmcnt(16)\n"
    "s_waitcnt lgkmcnt(0)\n"
    "s_barrier\n"
    "s_mul_i32 s88, s88, 0x9e3779b1\n"
    "s_add_u32 s88, s88, 0x7f4a7c15\n"
    "s_mul_i32 s89, s89, 0x85ebca75\n"
    "s_add_u32 s89, s89, 0x165667b1\n"
    "ds_read_b64_tr_b16 v[144:145], v243 offset:0\n"
    "ds_read_b64_tr_b16 v[146:147], v243 offset:4096\n"
    "ds_read_b64_tr_b16 v[148:149], v244 offset:0\n"
    "ds_read_b64_tr_b16 v[150:151], v244 offset:4096\n"
    "ds_read_b64_tr_b16 v[152:153], v243 offset:256\n"
    "ds_read_b64_tr_b16 v[154:155], v243 offset:4352\n"
    "ds_read_b64_tr_b16 v[156:157], v244 offset:256\n"
    "ds_read_b64_tr_b16 v[158:159], v244 offset:4352\n"
    "ds_read_b64_tr_b16 v[160:161], v243 offset:512\n"
    "ds_read_b64_tr_b16 v[162:163], v243 offset:4608\n"
    "ds_read_b64_tr_b16 v[164:165], v244 offset:512\n"
    "ds_read_b64_tr_b16 v[166:167], v244 offset:4608\n"
    "ds_read_b64_tr_b16 v[168:169], v243 offset:768\n"
    "ds_read_b64_tr_b16 v[170:171], v243 offset:4864\n"
    "ds_read_b64_tr_b16 v[172:173], v244 offset:768\n"
    "ds_read_b64_tr_b16 v[174:175], v244 offset:4864\n"
    "s_waitcnt vmcnt(12)\n"
    "s_mov_b64 vcc, s[4:5]\n"
    "v_cndmask_b32_dpp v224, v100, v96, vcc quad_perm:[1,0,3,2] row_mask:0xf bank_mask:0xf\n"
    "v_cndmask_b32_dpp v232, v108, v104, vcc quad_perm:[1,0,3,2] row_mask:0xf bank_mask:0xf\n"
    "v_cndmask_b32_dpp v225, v101, v97, vcc quad_perm:[1,0,3,2] row_mask:0xf bank_mask:0xf\n"
    "v_cndmask_b32_dpp v233, v109, v105, vcc quad_perm:[1,0,3,2] row_mask:0xf bank_mask:0xf\n"
    "v_cndmask_b32_dpp v226, v102, v98, vcc quad_perm:[1,0,3,2] row_mask:0xf bank_mask:0xf\n"
    "v_cndmask_b32_dpp v234, v110, v106, vcc quad_perm:[1,0,3,2] row_mask:0xf bank_mask:0xf\n"
    "v_cndmask_b32_dpp v227, v103, v99, vcc quad_perm:[1,0,3,2] row_mask:0xf bank_mask:0xf\n"
    "v_cndmask_b32_dpp v235, v111, v107, vcc quad_perm:[1,0,3,2] row_mask:0xf bank_mask:0xf\n"
    "s_mov_b64 vcc, s[6:7]\n"
    "v_cndmask_b32_dpp v228, v96, v100, vcc quad_perm:[1,0,3,2] row_mask:0xf bank_mask:0xf\n"
    "v_cndmask_b32_dpp v236, v104, v108, vcc quad_perm:[1,0,3,2] row_mask:0xf bank_mask:0xf\n"
    "v_cndmask_b32_dpp v229, v97, v101, vcc quad_perm:[1,0,3,2] row_mask:0xf bank_mask:0xf\n"
    "v_cndmask_b32_dpp v237, v105, v109, vcc quad_perm:[1,0,3,2] row_mask:0xf bank_mask:0xf\n"
    "v_cndmask_b32_dpp v230, v98, v102, vcc quad_perm:[1,0,3,2] row_mask:0xf bank_mask:0xf\n"
    "v_cndmask_b32_dpp v238, v106, v110, vcc quad_perm:[1,0,3,2] row_mask:0xf bank_mask:0xf\n"
    "v_cndmask_b32_dpp v231, v99, v103, vcc quad_perm:[1,0,3,2] row_mask:0xf bank_mask:0xf\n"
    "v_cndmask_b32_dpp v239, v107, v111, vcc quad_perm:[1,0,3,2] row_mask:0xf bank_mask:0xf\n"
    "s_mov_b64 vcc, s[8:9]\n"
    "v_cndmask_b32_dpp v96, v232, v224, vcc quad_perm:[2,3,0,1] row_mask:0xf bank_mask:0xf\n"
    "v_cndmask_b32_dpp v100, v236, v228, vcc quad_perm:[2,3,0,1] row_mask:0xf bank_mask:0xf\n"
    "v_cndmask_b32_dpp v97, v233, v225, vcc quad_perm:[2,3,0,1] row_mask:0xf bank_mask:0xf\n"
    "v_cndmask_b32_dpp v101, v237, v229, vcc quad_perm:[2,3,0,1] row_mask:0xf bank_mask:0xf\n"
    "v_cndmask_b32_dpp v98, v234, v226, vcc quad_perm:[2,3,0,1] row_mask:0xf bank_mask:0xf\n"
    "v_cndmask_b32_dpp v102, v238, v230, vcc quad_perm:[2,3,0,1] row_mask:0xf bank_mask:0xf\n"
    "v_cndmask_b32_dpp v99, v235, v227, vcc quad_perm:[2,3,0,1] row_mask:0xf bank_mask:0xf\n"
    "v_cndmask_b32_dpp v103, v239, v231, vcc quad_perm:[2,3,0,1] row_mask:0xf bank_mask:0xf\n"
    "s_mov_b64 vcc, s[10:11]\n"
    "v_cndmask_b32_dpp v104, v224, v232, vcc quad_perm:[2,3,0,1] row_mask:0xf bank_mask:0xf\n"
    "v_cndmask_b32_dpp v108, v228, v236, vcc quad_perm:[2,3,0,1] row_mask:0xf bank_mask:0xf\n"
    "v_cndmask_b32_dpp v105, v225, v233, vcc quad_perm:[2,3,0,1] row_mask:0xf bank_mask:0xf\n"
    "v_cndmask_b32_dpp v109, v229, v237, vcc quad_perm:[2,3,0,1] row_mask:0xf bank_mask:0xf\n"
    "v_cndmask_b32_dpp v106, v226, v234, vcc quad_perm:[2,3,0,1] row_mask:0xf bank_mask:0xf\n"
    "v_cndmask_b32_dpp v110, v230, v238, vcc quad_perm:[2,3,0,1] row_mask:0xf bank_mask:0xf\n"
    "v_cndmask_b32_dpp v107, v227, v235, vcc quad_perm:[2,3,0,1] row_mask:0xf bank_mask:0xf\n"
    "v_cndmask_b32_dpp v111, v231, v239, vcc quad_perm:[2,3,0,1] row_mask:0xf bank_mask:0xf\n"
    "s_waitcnt vmcnt(8)\n"
    "s_mov_b64 vcc, s[4:5]\n"
    "v_cndmask_b32_dpp v224, v116, v112, vcc quad_perm:[1,0,3,2] row_mask:0xf bank_mask:0xf\n"
    "v_cndmask_b32_dpp v232, v124, v120, vcc quad_perm:[1,0,3,2] row_mask:0xf bank_mask:0xf\n"
    "v_cndmask_b32_dpp v225, v117, v113, vcc quad_perm:[1,0,3,2] row_mask:0xf bank_mask:0xf\n"
    "v_cndmask_b32_dpp v233, v125, v121, vcc quad_perm:[1,0,3,2] row_mask:0xf bank_mask:0xf\n"
    "v_cndmask_b32_dpp v226, v118, v114, vcc quad_perm:[1,0,3,2] row_mask:0xf bank_mask:0xf\n"
    "v_cndmask_b32_dpp v234, v126, v122, vcc quad_perm:[1,0,3,2] row_mask:0xf bank_mask:0xf\n"
    "v_cndmask_b32_dpp v227, v119, v115, vcc quad_perm:[1,0,3,2] row_mask:0xf bank_mask:0xf\n"
    "v_cndmask_b32_dpp v235, v127, v123, vcc quad_perm:[1,0,3,2] row_mask:0xf bank_mask:0xf\n"
    "s_mov_b64 vcc, s[6:7]\n"
    "v_cndmask_b32_dpp v228, v112, v116, vcc quad_perm:[1,0,3,2] row_mask:0xf bank_mask:0xf\n"
    "v_cndmask_b32_dpp v236, v120, v124, vcc quad_perm:[1,0,3,2] row_mask:0xf bank_mask:0xf\n"
    "v_cndmask_b32_dpp v229, v113, v117, vcc quad_perm:[1,0,3,2] row_mask:0xf bank_mask:0xf\n"
    "v_cndmask_b32_dpp v237, v121, v125, vcc quad_perm:[1,0,3,2] row_mask:0xf bank_mask:0xf\n"
    "v_cndmask_b32_dpp v230, v114, v118, vcc quad_perm:[1,0,3,2] row_mask:0xf bank_mask:0xf\n"
    "v_cndmask_b32_dpp v238, v122, v126, vcc quad_perm:[1,0,3,2] row_mask:0xf bank_mask:0xf\n"
    "v_cndmask_b32_dpp v231, v115, v119, vcc quad_perm:[1,0,3,2] row_mask:0xf bank_mask:0xf\n"
    "v_cndmask_b32_dpp v239, v123, v127, vcc quad_perm:[1,0,3,2] row_mask:0xf bank_mask:0xf\n"
    "s_mov_b64 vcc, s[8:9]\n"
    "v_cndmask_b32_dpp v112, v232, v224, vcc quad_perm:[2,3,0,1] row_mask:0xf bank_mask:0xf\n"
    "v_cndmask_b32_dpp v116, v236, v228, vcc quad_perm:[2,3,0,1] row_mask:0xf bank_mask:0xf\n"
    "v_cndmask_b32_dpp v113, v233, v225, vcc quad_perm:[2,3,0,1] row_mask:0xf bank_mask:0xf\n"
    "v_cndmask_b32_dpp v117, v237, v229, vcc quad_perm:[2,3,0,1] row_mask:0xf bank_mask:0xf\n"
    "v_cndmask_b32_dpp v114, v234, v226, vcc quad_perm:[2,3,0,1] row_mask:0xf bank_mask:0xf\n"
    "v_cndmask_b32_dpp v118, v238, v230, vcc quad_perm:[2,3,0,1] row_mask:0xf bank_mask:0xf\n"
    "v_cndmask_b32_dpp v115, v235, v227, vcc quad_perm:[2,3,0,1] row_mask:0xf bank_mask:0xf\n"
    "v_cndmask_b32_dpp v119, v239, v231, vcc quad_perm:[2,3,0,1] row_mask:0xf bank_mask:0xf\n"
    "s_mov_b64 vcc, s[10:11]\n"
    "v_cndmask_b32_dpp v120, v224, v232, vcc quad_perm:[2,3,0,1] row_mask:0xf bank_mask:0xf\n"
    "v_cndmask_b32_dpp v124, v228, v236, vcc quad_perm:[2,3,0,1] row_mask:0xf bank_mask:0xf\n"
    "v_cndmask_b32_dpp v121, v225, v233, vcc quad_perm:[2,3,0,1] row_mask:0xf bank_mask:0xf\n"
    "v_cndmask_b32_dpp v125, v229, v237, vcc quad_perm:[2,3,0,1] row_mask:0xf bank_mask:0xf\n"
    "v_cndmask_b32_dpp v122, v226, v234, vcc quad_perm:[2,3,0,1] row_mask:0xf bank_mask:0xf\n"
    "v_cndmask_b32_dpp v126, v230, v238, vcc quad_perm:[2,3,0,1] row_mask:0xf bank_mask:0xf\n"
    "v_cndmask_b32_dpp v123, v227, v235, vcc quad_perm:[2,3,0,1] row_mask:0xf bank_mask:0xf\n"
    "v_cndmask_b32_dpp v127, v231, v239, vcc quad_perm:[2,3,0,1] row_mask:0xf bank_mask:0xf\n"
    "s_waitcnt vmcnt(4)\n"
    "s_mov_b64 vcc, s[4:5]\n"
    "v_cndmask_b32_dpp v224, v132, v128, vcc quad_perm:[1,0,3,2] row_mask:0xf bank_mask:0xf\n"
    "v_cndmask_b32_dpp v232, v140, v136, vcc quad_perm:[1,0,3,2] row_mask:0xf bank_mask:0xf\n"
    "v_cndmask_b32_dpp v225, v133, v129, vcc quad_perm:[1,0,3,2] row_mask:0xf bank_mask:0xf\n"
    "v_cndmask_b32_dpp v233, v141, v137, vcc quad_perm:[1,0,3,2] row_mask:0xf bank_mask:0xf\n"
    "v_cndmask_b32_dpp v226, v134, v130, vcc quad_perm:[1,0,3,2] row_mask:0xf bank_mask:0xf\n"
    "v_cndmask_b32_dpp v234, v142, v138, vcc quad_perm:[1,0,3,2] row_mask:0xf bank_mask:0xf\n"
    "v_cndmask_b32_dpp v227, v135, v131, vcc quad_perm:[1,0,3,2] row_mask:0xf bank_mask:0xf\n"
    "v_cndmask_b32_dpp v235, v143, v139, vcc quad_perm:[1,0,3,2] row_mask:0xf bank_mask:0xf\n"
    "s_mov_b64 vcc, s[6:7]\n"
    "v_cndmask_b32_dpp v228, v128, v132, vcc quad_perm:[1,0,3,2] row_mask:0xf bank_mask:0xf\n"
    "v_cndmask_b32_dpp v236, v136, v140, vcc quad_perm:[1,0,3,2] row_mask:0xf bank_mask:0xf\n"
    "v_cndmask_b32_dpp v229, v129, v133, vcc quad_perm:[1,0,3,2] row_mask:0xf bank_mask:0xf\n"
    "v_cndmask_b32_dpp v237, v137, v141, vcc quad_perm:[1,0,3,2] row_mask:0xf bank_mask:0xf\n"
    "v_cndmask_b32_dpp v230, v130, v134, vcc quad_perm:[1,0,3,2] row_mask:0xf bank_mask:0xf\n"
    "v_cndmask_b32_dpp v238, v138, v142, vcc quad_perm:[1,0,3,2] row_mask:0xf bank_mask:0xf\n"
    "v_cndmask_b32_dpp v231, v131, v135, vcc quad_perm:[1,0,3,2] row_mask:0xf bank_mask:0xf\n"
    "v_cndmask_b32_dpp v239, v139, v143, vcc quad_perm:[1,0,3,2] row_mask:0xf bank_mask:0xf\n"
    "s_mov_b64 vcc, s[8:9]\n"
    "v_cndmask_b32_dpp v128, v232, v224, vcc quad_perm:[2,3,0,1] row_mask:0xf bank_mask:0xf\n"
    "v_cndmask_b32_dpp v132, v236, v228, vcc quad_perm:[2,3,0,1] row_mask:0xf bank_mask:0xf\n"
    "v_cndmask_b32_dpp v129, v233, v225, vcc quad_perm:[2,3,0,1] row_mask:0xf bank_mask:0xf\n"
    "v_cndmask_b32_dpp v133, v237, v229, vcc quad_perm:[2,3,0,1] row_mask:0xf bank_mask:0xf\n"
    "v_cndmask_b32_dpp v130, v234, v226, vcc quad_perm:[2,3,0,1] row_mask:0xf bank_mask:0xf\n"
    "v_cndmask_b32_dpp v134, v238, v230, vcc quad_perm:[2,3,0,1] row_mask:0xf bank_mask:0xf\n"
    "v_cndmask_b32_dpp v131, v235, v227, vcc quad_perm:[2,3,0,1] row_mask:0xf bank_mask:0xf\n"
    "v_cndmask_b32_dpp v135, v239, v231, vcc quad_perm:[2,3,0,1] row_mask:0xf bank_mask:0xf\n"
    "s_mov_b64 vcc, s[10:11]\n"
    "v_cndmask_b32_dpp v136, v224, v232, vcc quad_perm:[2,3,0,1] row_mask:0xf bank_mask:0xf\n"
    "v_cndmask_b32_dpp v140, v228, v236, vcc quad_perm:[2,3,0,1] row_mask:0xf bank_mask:0xf\n"
    "v_cndmask_b32_dpp v137, v225, v233, vcc quad_perm:[2,3,0,1] row_mask:0xf bank_mask:0xf\n"
    "v_cndmask_b32_dpp v141, v229, v237, vcc quad_perm:[2,3,0,1] row_mask:0xf bank_mask:0xf\n"
    "v_cndmask_b32_dpp v138, v226, v234, vcc quad_perm:[2,3,0,1] row_mask:0xf bank_mask:0xf\n"
    "v_cndmask_b32_dpp v142, v230, v238, vcc quad_perm:[2,3,0,1] row_mask:0xf bank_mask:0xf\n"
    "v_cndmask_b32_dpp v139, v227, v235, vcc quad_perm:[2,3,0,1] row_mask:0xf bank_mask:0xf\n"
    "v_cndmask_b32_dpp v143, v231, v239, vcc quad_perm:[2,3,0,1] row_mask:0xf bank_mask:0xf\n"
    "s_waitcnt lgkmcnt(14)\n"
    "v_mfma_f32_16x16x32_bf16 v[0:3], v[144:147], v[96:99], 0\n"
    "s_add_u32 m0, s68, 32768\n"
    "v_mfma_f32_16x16x32_bf16 v[4:7], v[144:147], v[112:115], 0\n"
    "buffer_load_dwordx4 v250, s[40:43], s14 offen offset:0  lds\n"
    "v_add_u32 v243, 16384, v243\n"
    "v_add_u32 v244, 16384, v244\n"
    "v_mfma_f32_16x16x32_bf16 v[8:11], v[144:147], v[128:131], 0\n"
    "buffer_load_dwordx4 v251, s[40:43], s14 offen offset:1024  lds\n"
    "buffer_load_dwordx4 v252, s[40:43], s14 offen offset:2048  lds\n"
    "s_waitcnt lgkmcnt(12)\n"
    "v_mfma_f32_16x16x32_bf16 v[12:15], v[148:151], v[96:99], 0\n"
    "buffer_load_dwordx4 v253, s[40:43], s14 offen offset:3072  lds\n"
    "v_mfma_f32_16x16x32_bf16 v[16:19], v[148:151], v[112:115], 0\n"
    "s_waitcnt vmcnt(4)\n"
    "s_waitcnt lgkmcnt(0)\n"
    "s_barrier\n"
    "v_mfma_f32_16x16x32_bf16 v[20:23], v[148:151], v[128:131], 0\n"
    "ds_read_b64_tr_b16 v[144:145], v243 offset:8192\n"
    "buffer_load_dwordx4 v[176:179], v254, s[44:47], s24 offen offset:0 \n"
    "v_mfma_f32_16x16x32_bf16 v[24:27], v[152:155], v[96:99], 0\n"
    "ds_read_b64_tr_b16 v[146:147], v243 offset:12288\n"
    "buffer_load_dwordx4 v[180:183], v254, s[44:47], s24 offen offset:1024 \n"
    "v_mfma_f32_16x16x32_bf16 v[28:31], v[152:155], v[112:115], 0\n"
    "ds_read_b64_tr_b16 v[148:149], v244 offset:8192\n"
    "buffer_load_dwordx4 v[184:187], v254, s[44:47], s24 offen offset:2048 \n"
    "v_mfma_f32_16x16x32_bf16 v[32:35], v[152:155], v[128:131], 0\n"
    "ds_read_b64_tr_b16 v[150:151], v244 offset:12288\n"
    "buffer_load_dwordx4 v[188:191], v254, s[44:47], s24 offen offset:3072 \n"
    "v_mfma_f32_16x16x32_bf16 v[36:39], v[156:159], v[96:99], 0\n"
    "ds_read_b64_tr_b16 v[152:153], v243 offset:8448\n"
    "buffer_load_dwordx4 v[192:195], v254, s[44:47], s25 offen offset:0 \n"
    "v_mfma_f32_16x16x32_bf16 v[40:43], v[156:159], v[112:115], 0\n"
    "ds_read_b64_tr_b16 v[154:155], v243 offset:12544\n"
    "buffer_load_dwordx4 v[196:199], v254, s[44:47], s25 offen offset:1024 \n"
    "v_mfma_f32_16x16x32_bf16 v[44:47], v[156:159], v[128:131], 0\n"
    "ds_read_b64_tr_b16 v[156:157], v244 offset:8448\n"
    "buffer_load_dwordx4 v[200:203], v254, s[44:47], s25 offen offset:2048 \n"
    "v_mfma_f32_16x16x32_bf16 v[48:51], v[160:163], v[96:99], 0\n"
    "ds_read_b64_tr_b16 v[158:159], v244 offset:12544\n"
    "buffer_load_dwordx4 v[204:207], v254, s[44:47], s25 offen offset:3072 \n"
    "v_mfma_f32_16x16x32_bf16 v[52:55], v[160:163], v[112:115], 0\n"
    "buffer_load_dwordx4 v[208:211], v254, s[44:47], s26 offen offset:0 \n"
    "buffer_load_dwordx4 v[212:215], v254, s[44:47], s26 offen offset:1024 \n"
    "v_mfma_f32_16x16x32_bf16 v[56:59], v[160:163], v[128:131], 0\n"
    "ds_read_b64_tr_b16 v[160:161], v243 offset:8704\n"
    "buffer_load_dwordx4 v[216:219], v254, s[44:47], s26 offen offset:2048 \n"
    "v_mfma_f32_16x16x32_bf16 v[60:63], v[164:167], v[96:99], 0\n"
    "ds_read_b64_tr_b16 v[162:163], v243 offset:12800\n"
    "buffer_load_dwordx4 v[220:223], v254, s[44:47], s26 offen offset:3072 \n"
    "v_mfma_f32_16x16x32_bf16 v[64:67], v[164:167], v[112:115], 0\n"
    "s_add_u32 m0, s68, 49152\n"
    "v_mfma_f32_16x16x32_bf16 v[68:71], v[164:167], v[128:131], 0\n"
    "ds_read_b64_tr_b16 v[164:165], v244 offset:8704\n"
    "buffer_load_dwordx4 v250, s[40:43], s15 offen offset:0  lds\n"
    "v_mfma_f32_16x16x32_bf16 v[72:75], v[168:171], v[96:99], 0\n"
    "ds_read_b64_tr_b16 v[166:167], v244 offset:12800\n"
    "buffer_load_dwordx4 v251, s[40:43], s15 offen offset:1024  lds\n"
    "v_mfma_f32_16x16x32_bf16 v[76:79], v[168:171], v[112:115], 0\n"
    "buffer_load_dwordx4 v252, s[40:43], s15 offen offset:2048  lds\n"
    "buffer_load_dwordx4 v253, s[40:43], s15 offen offset:3072  lds\n"
    "v_mfma_f32_16x16x32_bf16 v[80:83], v[168:171], v[128:131], 0\n"
    "s_add_u32 m0, s68, 65536\n"
    "ds_read_b64_tr_b16 v[168:169], v243 offset:8960\n"
    "v_mfma_f32_16x16x32_bf16 v[84:87], v[172:175], v[96:99], 0\n"
    "ds_read_b64_tr_b16 v[170:171], v243 offset:13056\n"
    "buffer_load_dwordx4 v250, s[40:43], s16 offen offset:0  lds\n"
    "v_mfma_f32_16x16x32_bf16 v[88:91], v[172:175], v[112:115], 0\n"
    "buffer_load_dwordx4 v251, s[40:43], s16 offen offset:1024  lds\n"
    "buffer_load_dwordx4 v252, s[40:43], s16 offen offset:2048  lds\n"
    "v_mfma_f32_16x16x32_bf16 v[92:95], v[172:175], v[128:131], 0\n"
    "s_waitcnt lgkmcnt(6)\n"
    "ds_read_b64_tr_b16 v[172:173], v244 offset:8960\n"
    "v_mfma_f32_16x16x32_bf16 v[0:3], v[144:147], v[100:103], v[0:3]\n"
    "ds_read_b64_tr_b16 v[174:175], v244 offset:13056\n"
    "buffer_load_dwordx4 v253, s[40:43], s16 offen offset:3072  lds\n"
    "v_mfma_f32_16x16x32_bf16 v[4:7], v[144:147], v[116:119], v[4:7]\n"
    "s_add_u32 m0, s68, 0\n"
    "v_mfma_f32_16x16x32_bf16 v[8:11], v[144:147], v[132:135], v[8:11]\n"
    "ds_read_b64_tr_b16 v[144:145], v243 offset:0\n"
    "buffer_load_dwordx4 v250, s[40:43], s17 offen offset:0  lds\n"
    "v_mfma_f32_16x16x32_bf16 v[12:15], v[148:151], v[100:103], v[12:15]\n"
    "ds_read_b64_tr_b16 v[146:147], v243 offset:4096\n"
    "buffer_load_dwordx4 v251, s[40:43], s17 offen offset:1024  lds\n"
    "v_mfma_f32_16x16x32_bf16 v[16:19], v[148:151], v[116:119], v[16:19]\n"
    "buffer_load_dwordx4 v252, s[40:43], s17 offen offset:2048  lds\n"
    "buffer_load_dwordx4 v253, s[40:43], s17 offen offset:3072  lds\n"
    "v_mfma_f32_16x16x32_bf16 v[20:23], v[148:151], v[132:135], v[20:23]\n"
    "ds_read_b64_tr_b16 v[148:149], v244 offset:0\n"
    "buffer_load_dwordx4 v[96:99], v254, s[44:47], s27 offen offset:0 \n"
    "v_mfma_f32_16x16x32_bf16 v[24:27], v[152:155], v[100:103], v[24:27]\n"
    "ds_read_b64_tr_b16 v[150:151], v244 offset:4096\n"
    "buffer_load_dwordx4 v[112:115], v254, s[44:47], s28 offen offset:0 \n"
    "v_mfma_f32_16x16x32_bf16 v[28:31], v[152:155], v[116:119], v[28:31]\n"
    "buffer_load_dwordx4 v[128:131], v254, s[44:47], s29 offen offset:0 \n"
    "v_mfma_f32_16x16x32_bf16 v[32:35], v[152:155], v[132:135], v[32:35]\n"
    "ds_read_b64_tr_b16 v[152:153], v243 offset:256\n"
    "v_mfma_f32_16x16x32_bf16 v[36:39], v[156:159], v[100:103], v[36:39]\n"
    "ds_read_b64_tr_b16 v[154:155], v243 offset:4352\n"
    "v_mfma_f32_16x16x32_bf16 v[40:43], v[156:159], v[116:119], v[40:43]\n"
    "v_mfma_f32_16x16x32_bf16 v[44:47], v[156:159], v[132:135], v[44:47]\n"
    "s_waitcnt lgkmcnt(6)\n"
    "ds_read_b64_tr_b16 v[156:157], v244 offset:256\n"
    "v_mfma_f32_16x16x32_bf16 v[48:51], v[160:163], v[100:103], v[48:51]\n"
    "ds_read_b64_tr_b16 v[158:159], v244 offset:4352\n"
    "v_mfma_f32_16x16x32_bf16 v[52:55], v[160:163], v[116:119], v[52:55]\n"
    "v_mfma_f32_16x16x32_bf16 v[56:59], v[160:163], v[132:135], v[56:59]\n"
    "ds_read_b64_tr_b16 v[160:161], v243 offset:512\n"
    "v_mfma_f32_16x16x32_bf16 v[60:63], v[164:167], v[100:103], v[60:63]\n"
    "ds_read_b64_tr_b16 v[162:163], v243 offset:4608\n"
    "v_mfma_f32_16x16x32_bf16 v[64:67], v[164:167], v[116:119], v[64:67]\n"
    "s_waitcnt vmcnt(23)\n"
    "s_mov_b64 vcc, s[4:5]\n"
    "v_cndmask_b32_dpp v224, v180, v176, vcc quad_perm:[1,0,3,2] row_mask:0xf bank_mask:0xf\n"
    "v_cndmask_b32_dpp v232, v188, v184, vcc quad_perm:[1,0,3,2] row_mask:0xf bank_mask:0xf\n"
    "v_cndmask_b32_dpp v225, v181, v177, vcc quad_perm:[1,0,3,2] row_mask:0xf bank_mask:0xf\n"
    "v_mfma_f32_16x16x32_bf16 v[68:71], v[164:167], v[132:135], v[68:71]\n"
    "ds_read_b64_tr_b16 v[164:165], v244 offset:512\n"
    "v_cndmask_b32_dpp v233, v189, v185, vcc quad_perm:[1,0,3,2] row_mask:0xf bank_mask:0xf\n"
    "v_cndmask_b32_dpp v226, v182, v178, vcc quad_perm:[1,0,3,2] row_mask:0xf bank_mask:0xf\n"
    "v_cndmask_b32_dpp v234, v190, v186, vcc quad_perm:[1,0,3,2] row_mask:0xf bank_mask:0xf\n"
    "v_mfma_f32_16x16x32_bf16 v[72:75], v[168:171], v[100:103], v[72:75]\n"
    "ds_read_b64_tr_b16 v[166:167], v244 offset:4608\n"
    "v_cndmask_b32_dpp v227, v183, v179, vcc quad_perm:[1,0,3,2] row_mask:0xf bank_mask:0xf\n"
    "v_cndmask_b32_dpp v235, v191, v187, vcc quad_perm:[1,0,3,2] row_mask:0xf bank_mask:0xf\n"
    "v_mfma_f32_16x16x32_bf16 v[76:79], v[168:171], v[116:119], v[76:79]\n"
    "s_mov_b64 vcc, s[6:7]\n"
    "v_cndmask_b32_dpp v228, v176, v180, vcc quad_perm:[1,0,3,2] row_mask:0xf bank_mask:0xf\n"
    "v_cndmask_b32_dpp v236, v184, v188, vcc quad_perm:[1,0,3,2] row_mask:0xf bank_mask:0xf\n"
    "v_cndmask_b32_dpp v229, v177, v181, vcc quad_perm:[1,0,3,2] row_mask:0xf bank_mask:0xf\n"
    "v_cndmask_b32_dpp v237, v185, v189, vcc quad_perm:[1,0,3,2] row_mask:0xf bank_mask:0xf\n"
    "v_mfma_f32_16x16x32_bf16 v[80:83], v[168:171], v[132:135], v[80:83]\n"
    "ds_read_b64_tr_b16 v[168:169], v243 offset:768\n"
    "v_cndmask_b32_dpp v230, v178, v182, vcc quad_perm:[1,0,3,2] row_mask:0xf bank_mask:0xf\n"
    "v_cndmask_b32_dpp v238, v186, v190, vcc quad_perm:[1,0,3,2] row_mask:0xf bank_mask:0xf\n"
    "v_cndmask_b32_dpp v231, v179, v183, vcc quad_perm:[1,0,3,2] row_mask:0xf bank_mask:0xf\n"
    "v_mfma_f32_16x16x32_bf16 v[84:87], v[172:175], v[100:103], v[84:87]\n"
    "ds_read_b64_tr_b16 v[170:171], v243 offset:4864\n"
    "v_cndmask_b32_dpp v239, v187, v191, vcc quad_perm:[1,0,3,2] row_mask:0xf bank_mask:0xf\n"
    "v_mfma_f32_16x16x32_bf16 v[88:91], v[172:175], v[116:119], v[88:91]\n"
    "s_mov_b64 vcc, s[8:9]\n"
    "v_cndmask_b32_dpp v176, v232, v224, vcc quad_perm:[2,3,0,1] row_mask:0xf bank_mask:0xf\n"
    "v_cndmask_b32_dpp v180, v236, v228, vcc quad_perm:[2,3,0,1] row_mask:0xf bank_mask:0xf\n"
    "v_cndmask_b32_dpp v177, v233, v225, vcc quad_perm:[2,3,0,1] row_mask:0xf bank_mask:0xf\n"
    "v_cndmask_b32_dpp v181, v237, v229, vcc quad_perm:[2,3,0,1] row_mask:0xf bank_mask:0xf\n"
    "v_mfma_f32_16x16x32_bf16 v[92:95], v[172:175], v[132:135], v[92:95]\n"
    "s_waitcnt lgkmcnt(10)\n"
    "ds_read_b64_tr_b16 v[172:173], v244 offset:768\n"
    "v_cndmask_b32_dpp v178, v234, v226, vcc quad_perm:[2,3,0,1] row_mask:0xf bank_mask:0xf\n"
    "v_cndmask_b32_dpp v182, v238, v230, vcc quad_perm:[2,3,0,1] row_mask:0xf bank_mask:0xf\n"
    "v_mfma_f32_16x16x32_bf16 v[0:3], v[144:147], v[104:107], v[0:3]\n"
    "ds_read_b64_tr_b16 v[174:175], v244 offset:4864\n"
    "buffer_load_dwordx4 v[100:103], v254, s[44:47], s27 offen offset:1024 \n"
    "v_cndmask_b32_dpp v179, v235, v227, vcc quad_perm:[2,3,0,1] row_mask:0xf bank_mask:0xf\n"
    "v_mfma_f32_16x16x32_bf16 v[4:7], v[144:147], v[120:123], v[4:7]\n"
    "buffer_load_dwordx4 v[116:119], v254, s[44:47], s28 offen offset:1024 \n"
    "v_add_u32 v243, 16384, v243\n"
    "v_add_u32 v244, 16384, v244\n"
    "v_cndmask_b32_dpp v183, v239, v231, vcc quad_perm:[2,3,0,1] row_mask:0xf bank_mask:0xf\n"
    "v_mfma_f32_16x16x32_bf16 v[8:11], v[144:147], v[136:139], v[8:11]\n"
    "s_mov_b64 vcc, s[10:11]\n"
    "buffer_load_dwordx4 v[132:135], v254, s[44:47], s29 offen offset:1024 \n"
    "v_cndmask_b32_dpp v184, v224, v232, vcc quad_perm:[2,3,0,1] row_mask:0xf bank_mask:0xf\n"
    "v_cndmask_b32_dpp v188, v228, v236, vcc quad_perm:[2,3,0,1] row_mask:0xf bank_mask:0xf\n"
    "v_mfma_f32_16x16x32_bf16 v[12:15], v[148:151], v[104:107], v[12:15]\n"
    "v_cndmask_b32_dpp v185, v225, v233, vcc quad_perm:[2,3,0,1] row_mask:0xf bank_mask:0xf\n"
    "v_cndmask_b32_dpp v189, v229, v237, vcc quad_perm:[2,3,0,1] row_mask:0xf bank_mask:0xf\n"
    "v_cndmask_b32_dpp v186, v226, v234, vcc quad_perm:[2,3,0,1] row_mask:0xf bank_mask:0xf\n"
    "v_cndmask_b32_dpp v190, v230, v238, vcc quad_perm:[2,3,0,1] row_mask:0xf bank_mask:0xf\n"
    "v_mfma_f32_16x16x32_bf16 v[16:19], v[148:151], v[120:123], v[16:19]\n"
    "s_cmp_eq_u32 s72, 0\n"
    "s_cbranch_scc1 Lqr%=\n"
    "s_setprio 3\n"
    "Lqr%=:\n"
    "s_waitcnt lgkmcnt(0)\n"
    "s_barrier\n"
    "v_cndmask_b32_dpp v187, v227, v235, vcc quad_perm:[2,3,0,1] row_mask:0xf bank_mask:0xf\n"
    "v_cndmask_b32_dpp v191, v231, v239, vcc quad_perm:[2,3,0,1] row_mask:0xf bank_mask:0xf\n"
    "v_mfma_f32_16x16x32_bf16 v[20:23], v[148:151], v[136:139], v[20:23]\n"
    "s_waitcnt vmcnt(22)\n"
    "s_mov_b64 vcc, s[4:5]\n"
    "ds_read_b64_tr_b16 v[144:145], v243 offset:8192\n"
    "v_cndmask_b32_dpp v224, v196, v192, vcc quad_perm:[1,0,3,2] row_mask:0xf bank_mask:0xf\n"
    "v_mfma_f32_16x16x32_bf16 v[24:27], v[152:155], v[104:107], v[24:27]\n"
    "s_add_u32 m0, s68, 16384\n"
    "ds_read_b64_tr_b16 v[146:147], v243 offset:12288\n"
    "v_cndmask_b32_dpp v232, v204, v200, vcc quad_perm:[1,0,3,2] row_mask:0xf bank_mask:0xf\n"
    "v_cndmask_b32_dpp v225, v197, v193, vcc quad_perm:[1,0,3,2] row_mask:0xf bank_mask:0xf\n"
    "v_mfma_f32_16x16x32_bf16 v[28:31], v[152:155], v[120:123], v[28:31]\n"
    "ds_read_b64_tr_b16 v[148:149], v244 offset:8192\n"
    "buffer_load_dwordx4 v250, s[40:43], s18 offen offset:0  lds\n"
    "v_cndmask_b32_dpp v233, v205, v201, vcc quad_perm:[1,0,3,2] row_mask:0xf bank_mask:0xf\n"
    "v_mfma_f32_16x16x32_bf16 v[32:35], v[152:155], v[136:139], v[32:35]\n"
    "ds_read_b64_tr_b16 v[150:151], v244 offset:12288\n"
    "buffer_load_dwordx4 v251, s[40:43], s18 offen offset:1024  lds\n"
    "v_cndmask_b32_dpp v226, v198, v194, vcc quad_perm:[1,0,3,2] row_mask:0xf bank_mask:0xf\n"
    "v_mfma_f32_16x16x32_bf16 v[36:39], v[156:159], v[104:107], v[36:39]\n"
    "ds_read_b64_tr_b16 v[152:153], v243 offset:8448\n"
    "buffer_load_dwordx4 v252, s[40:43], s18 offen offset:2048  lds\n"
    "v_cndmask_b32_dpp v234, v206, v202, vcc quad_perm:[1,0,3,2] row_mask:0xf bank_mask:0xf\n"
    "v_mfma_f32_16x16x32_bf16 v[40:43], v[156:159], v[120:123], v[40:43]\n"
    "ds_read_b64_tr_b16 v[154:155], v243 offset:12544\n"
    "buffer_load_dwordx4 v253, s[40:43], s18 offen offset:3072  lds\n"
    "v_cndmask_b32_dpp v227, v199, v195, vcc quad_perm:[1,0,3,2] row_mask:0xf bank_mask:0xf\n"
    "v_mfma_f32_16x16x32_bf16 v[44:47], v[156:159], v[136:139], v[44:47]\n"
    "ds_read_b64_tr_b16 v[156:157], v244 offset:8448\n"
    "v_cndmask_b32_dpp v235, v207, v203, vcc quad_perm:[1,0,3,2] row_mask:0xf bank_mask:0xf\n"
    "v_mfma_f32_16x16x32_bf16 v[48:51], v[160:163], v[104:107], v[48:51]\n"
    "s_mov_b64 vcc, s[6:7]\n"
    "ds_read_b64_tr_b16 v[158:159], v244 offset:12544\n"
    "v_cndmask_b32_dpp v228, v192, v196, vcc quad_perm:[1,0,3,2] row_mask:0xf bank_mask:0xf\n"
    "v_cndmask_b32_dpp v236, v200, v204, vcc quad_perm:[1,0,3,2] row_mask:0xf bank_mask:0xf\n"
    "v_mfma_f32_16x16x32_bf16 v[52:55], v[160:163], v[120:123], v[52:55]\n"
    "v_cndmask_b32_dpp v229, v193, v197, vcc quad_perm:[1,0,3,2] row_mask:0xf bank_mask:0xf\n"
    "v_cndmask_b32_dpp v237, v201, v205, vcc quad_perm:[1,0,3,2] row_mask:0xf bank_mask:0xf\n"
    "v_cndmask_b32_dpp v230, v194, v198, vcc quad_perm:[1,0,3,2] row_mask:0xf bank_mask:0xf\n"
    "v_cndmask_b32_dpp v238, v202, v206, vcc quad_perm:[1,0,3,2] row_mask:0xf bank_mask:0xf\n"
    "v_mfma_f32_16x16x32_bf16 v[56:59], v[160:163], v[136:139], v[56:59]\n"
    "ds_read_b64_tr_b16 v[160:161], v243 offset:8704\n"
    "v_cndmask_b32_dpp v231, v195, v199, vcc quad_perm:[1,0,3,2] row_mask:0xf bank_mask:0xf\n"
    "v_cndmask_b32_dpp v239, v203, v207, vcc quad_perm:[1,0,3,2] row_mask:0xf bank_mask:0xf\n"
    "v_mfma_f32_16x16x32_bf16 v[60:63], v[164:167], v[104:107], v[60:63]\n"
    "s_mov_b64 vcc, s[8:9]\n"
    "ds_read_b64_tr_b16 v[162:163], v243 offset:12800\n"
    "v_cndmask_b32_dpp v192, v232, v224, vcc quad_perm:[2,3,0,1] row_mask:0xf bank_mask:0xf\n"
    "v_cndmask_b32_dpp v196, v236, v228, vcc quad_perm:[2,3,0,1] row_mask:0xf bank_mask:0xf\n"
    "v_mfma_f32_16x16x32_bf16 v[64:67], v[164:167], v[120:123], v[64:67]\n"
    "v_cndmask_b32_dpp v193, v233, v225, vcc quad_perm:[2,3,0,1] row_mask:0xf bank_mask:0xf\n"
    "v_cndmask_b32_dpp v197, v237, v229, vcc quad_perm:[2,3,0,1] row_mask:0xf bank_mask:0xf\n"
    "v_cndmask_b32_dpp v194, v234, v226, vcc quad_perm:[2,3,0,1] row_mask:0xf bank_mask:0xf\n"
    "v_cndmask_b32_dpp v198, v238, v230, vcc quad_perm:[2,3,0,1] row_mask:0xf bank_mask:0xf\n"
    "v_mfma_f32_16x16x32_bf16 v[68:71], v[164:167], v[136:139], v[68:71]\n"
    "ds_read_b64_tr_b16 v[164:165], v244 offset:8704\n"
    "v_cndmask_b32_dpp v195, v235, v227, vcc quad_perm:[2,3,0,1] row_mask:0xf bank_mask:0xf\n"
    "v_cndmask_b32_dpp v199, v239, v231, vcc quad_perm:[2,3,0,1] row_mask:0xf bank_mask:0xf\n"
    "v_mfma_f32_16x16x32_bf16 v[72:75], v[168:171], v[104:107], v[72:75]\n"
    "s_mov_b64 vcc, s[10:11]\n"
    "ds_read_b64_tr_b16 v[166:167], v244 offset:12800\n"
    "v_cndmask_b32_dpp v200, v224, v232, vcc quad_perm:[2,3,0,1] row_mask:0xf bank_mask:0xf\n"
    "v_cndmask_b32_dpp v204, v228, v236, vcc quad_perm:[2,3,0,1] row_mask:0xf bank_mask:0xf\n"
    "v_mfma_f32_16x16x32_bf16 v[76:79], v[168:171], v[120:123], v[76:79]\n"
    "v_cndmask_b32_dpp v201, v225, v233, vcc quad_perm:[2,3,0,1] row_mask:0xf bank_mask:0xf\n"
    "v_cndmask_b32_dpp v205, v229, v237, vcc quad_perm:[2,3,0,1] row_mask:0xf bank_mask:0xf\n"
    "v_cndmask_b32_dpp v202, v226, v234, vcc quad_perm:[2,3,0,1] row_mask:0xf bank_mask:0xf\n"
    "v_cndmask_b32_dpp v206, v230, v238, vcc quad_perm:[2,3,0,1] row_mask:0xf bank_mask:0xf\n"
    "v_mfma_f32_16x16x32_bf16 v[80:83], v[168:171], v[136:139], v[80:83]\n"
    "ds_read_b64_tr_b16 v[168:169], v243 offset:8960\n"
    "v_cndmask_b32_dpp v203, v227, v235, vcc quad_perm:[2,3,0,1] row_mask:0xf bank_mask:0xf\n"
    "v_cndmask_b32_dpp v207, v231, v239, vcc quad_perm:[2,3,0,1] row_mask:0xf bank_mask:0xf\n"
    "v_mfma_f32_16x16x32_bf16 v[84:87], v[172:175], v[104:107], v[84:87]\n"
    "s_waitcnt vmcnt(22)\n"
    "s_mov_b64 vcc, s[4:5]\n"
    "ds_read_b64_tr_b16 v[170:171], v243 offset:13056\n"
    "v_cndmask_b32_dpp v224, v212, v208, vcc quad_perm:[1,0,3,2] row_mask:0xf bank_mask:0xf\n"
    "v_mfma_f32_16x16x32_bf16 v[88:91], v[172:175], v[120:123], v[88:91]\n"
    "v_cndmask_b32_dpp v232, v220, v216, vcc quad_perm:[1,0,3,2] row_mask:0xf bank_mask:0xf\n"
    "v_cndmask_b32_dpp v225, v213, v209, vcc quad_perm:[1,0,3,2] row_mask:0xf bank_mask:0xf\n"
    "v_cndmask_b32_dpp v233, v221, v217, vcc quad_perm:[1,0,3,2] row_mask:0xf bank_mask:0xf\n"
    "v_cndmask_b32_dpp v226, v214, v210, vcc quad_perm:[1,0,3,2] row_mask:0xf bank_mask:0xf\n"
    "v_mfma_f32_16x16x32_bf16 v[92:95], v[172:175], v[136:139], v[92:95]\n"
    "s_waitcnt lgkmcnt(6)\n"
    "ds_read_b64_tr_b16 v[172:173], v244 offset:8960\n"
    "v_cndmask_b32_dpp v234, v222, v218, vcc quad_perm:[1,0,3,2] row_mask:0xf bank_mask:0xf\n"
    "v_cndmask_b32_dpp v227, v215, v211, vcc quad_perm:[1,0,3,2] row_mask:0xf bank_mask:0xf\n"
    "v_mfma_f32_16x16x32_bf16 v[0:3], v[144:147], v[108:111], v[0:3]\n"
    "ds_read_b64_tr_b16 v[174:175], v244 offset:13056\n"
    "buffer_load_dwordx4 v[104:107], v254, s[44:47], s27 offen offset:2048 \n"
    "v_cndmask_b32_dpp v235, v223, v219, vcc quad_perm:[1,0,3,2] row_mask:0xf bank_mask:0xf\n"
    "v_mfma_f32_16x16x32_bf16 v[4:7], v[144:147], v[124:127], v[4:7]\n"
    "s_mov_b64 vcc, s[6:7]\n"
    "buffer_load_dwordx4 v[120:123], v254, s[44:47], s28 offen offset:2048 \n"
    "buffer_load_dwordx4 v[136:139], v254, s[44:47], s29 offen offset:2048 \n"
    "v_mfma_f32_16x16x32_bf16 v[8:11], v[144:147], v[140:143], v[8:11]\n"
    "ds_read_b64_tr_b16 v[144:145], v243 offset:0\n"
    "v_cndmask_b32_dpp v228, v208, v212, vcc quad_perm:[1,0,3,2] row_mask:0xf bank_mask:0xf\n"
    "v_cndmask_b32_dpp v236, v216, v220, vcc quad_perm:[1,0,3,2] row_mask:0xf bank_mask:0xf\n"
    "v_cndmask_b32_dpp v229, v209, v213, vcc quad_perm:[1,0,3,2] row_mask:0xf bank_mask:0xf\n"
    "v_mfma_f32_16x16x32_bf16 v[12:15], v[148:151], v[108:111], v[12:15]\n"
    "ds_read_b64_tr_b16 v[146:147], v243 offset:4096\n"
    "v_cndmask_b32_dpp v237, v217, v221, vcc quad_perm:[1,0,3,2] row_mask:0xf bank_mask:0xf\n"
    "v_cndmask_b32_dpp v230, v210, v214, vcc quad_perm:[1,0,3,2] row_mask:0xf bank_mask:0xf\n"
    "v_cndmask_b32_dpp v238, v218, v222, vcc quad_perm:[1,0,3,2] row_mask:0xf bank_mask:0xf\n"
    "v_mfma_f32_16x16x32_bf16 v[16:19], v[148:151], v[124:127], v[16:19]\n"
    "v_cndmask_b32_dpp v231, v211, v215, vcc quad_perm:[1,0,3,2] row_mask:0xf bank_mask:0xf\n"
    "v_cndmask_b32_dpp v239, v219, v223, vcc quad_perm:[1,0,3,2] row_mask:0xf bank_mask:0xf\n"
    "v_mfma_f32_16x16x32_bf16 v[20:23], v[148:151], v[140:143], v[20:23]\n"
    "s_mov_b64 vcc, s[8:9]\n"
    "ds_read_b64_tr_b16 v[148:149], v244 offset:0\n"
    "v_cndmask_b32_dpp v208, v232, v224, vcc quad_perm:[2,3,0,1] row_mask:0xf bank_mask:0xf\n"
    "v_cndmask_b32_dpp v212, v236, v228, vcc quad_perm:[2,3,0,1] row_mask:0xf bank_mask:0xf\n"
    "v_mfma_f32_16x16x32_bf16 v[24:27], v[152:155], v[108:111], v[24:27]\n"
    "ds_read_b64_tr_b16 v[150:151], v244 offset:4096\n"
    "v_cndmask_b32_dpp v209, v233, v225, vcc quad_perm:[2,3,0,1] row_mask:0xf bank_mask:0xf\n"
    "v_cndmask_b32_dpp v213, v237, v229, vcc quad_perm:[2,3,0,1] row_mask:0xf bank_mask:0xf\n"
    "v_cndmask_b32_dpp v210, v234, v226, vcc quad_perm:[2,3,0,1] row_mask:0xf bank_mask:0xf\n"
    "v_mfma_f32_16x16x32_bf16 v[28:31], v[152:155], v[124:127], v[28:31]\n"
    "v_cndmask_b32_dpp v214, v238, v230, vcc quad_perm:[2,3,0,1] row_mask:0xf bank_mask:0xf\n"
    "v_cndmask_b32_dpp v211, v235, v227, vcc quad_perm:[2,3,0,1] row_mask:0xf bank_mask:0xf\n"
    "v_cndmask_b32_dpp v215, v239, v231, vcc quad_perm:[2,3,0,1] row_mask:0xf bank_mask:0xf\n"
    "v_mfma_f32_16x16x32_bf16 v[32:35], v[152:155], v[140:143], v[32:35]\n"
    "s_mov_b64 vcc, s[10:11]\n"
    "ds_read_b64_tr_b16 v[152:153], v243 offset:256\n"
    "v_cndmask_b32_dpp v216, v224, v232, vcc quad_perm:[2,3,0,1] row_mask:0xf bank_mask:0xf\n"
    "v_cndmask_b32_dpp v220, v228, v236, vcc quad_perm:[2,3,0,1] row_mask:0xf bank_mask:0xf\n"
    "v_mfma_f32_16x16x32_bf16 v[36:39], v[156:159], v[108:111], v[36:39]\n"
    "ds_read_b64_tr_b16 v[154:155], v243 offset:4352\n"
    "v_cndmask_b32_dpp v217, v225, v233, vcc quad_perm:[2,3,0,1] row_mask:0xf bank_mask:0xf\n"
    "v_cndmask_b32_dpp v221, v229, v237, vcc quad_perm:[2,3,0,1] row_mask:0xf bank_mask:0xf\n"
    "v_cndmask_b32_dpp v218, v226, v234, vcc quad_perm:[2,3,0,1] row_mask:0xf bank_mask:0xf\n"
    "v_mfma_f32_16x16x32_bf16 v[40:43], v[156:159], v[124:127], v[40:43]\n"
    "v_cndmask_b32_dpp v222, v230, v238, vcc quad_perm:[2,3,0,1] row_mask:0xf bank_mask:0xf\n"
    "v_cndmask_b32_dpp v219, v227, v235, vcc quad_perm:[2,3,0,1] row_mask:0xf bank_mask:0xf\n"
    "v_cndmask_b32_dpp v223, v231, v239, vcc quad_perm:[2,3,0,1] row_mask:0xf bank_mask:0xf\n"
    "v_mfma_f32_16x16x32_bf16 v[44:47], v[156:159], v[140:143], v[44:47]\n"
    "s_waitcnt lgkmcnt(6)\n"
    "ds_read_b64_tr_b16 v[156:157], v244 offset:256\n"
    "v_mfma_f32_16x16x32_bf16 v[48:51], v[160:163], v[108:111], v[48:51]\n"
    "ds_read_b64_tr_b16 v[158:159], v244 offset:4352\n"
    "v_mfma_f32_16x16x32_bf16 v[52:55], v[160:163], v[124:127], v[52:55]\n"
    "v_mfma_f32_16x16x32_bf16 v[56:59], v[160:163], v[140:143], v[56:59]\n"
    "ds_read_b64_tr_b16 v[160:161], v243 offset:512\n"
    "v_mfma_f32_16x16x32_bf16 v[60:63], v[164:167], v[108:111], v[60:63]\n"
    "ds_read_b64_tr_b16 v[162:163], v243 offset:4608\n"
    "v_mfma_f32_16x16x32_bf16 v[64:67], v[164:167], v[124:127], v[64:67]\n"
    "v_mfma_f32_16x16x32_bf16 v[68:71], v[164:167], v[140:143], v[68:71]\n"
    "ds_read_b64_tr_b16 v[164:165], v244 offset:512\n"
    "v_mfma_f32_16x16x32_bf16 v[72:75], v[168:171], v[108:111], v[72:75]\n"
    "ds_read_b64_tr_b16 v[166:167], v244 offset:4608\n"
    "v_mfma_f32_16x16x32_bf16 v[76:79], v[168:171], v[124:127], v[76:79]\n"
    "v_mfma_f32_16x16x32_bf16 v[80:83], v[168:171], v[140:143], v[80:83]\n"
    "ds_read_b64_tr_b16 v[168:169], v243 offset:768\n"
    "v_mfma_f32_16x16x32_bf16 v[84:87], v[172:175], v[108:111], v[84:87]\n"
    "ds_read_b64_tr_b16 v[170:171], v243 offset:4864\n"
    "v_mfma_f32_16x16x32_bf16 v[88:91], v[172:175], v[124:127], v[88:91]\n"
    "v_mfma_f32_16x16x32_bf16 v[92:95], v[172:175], v[140:143], v[92:95]\n"
    "s_waitcnt lgkmcnt(10)\n"
    "ds_read_b64_tr_b16 v[172:173], v244 offset:768\n"
    "v_mfma_f32_16x16x32_bf16 v[0:3], v[144:147], v[176:179], v[0:3]\n"
    "ds_read_b64_tr_b16 v[174:175], v244 offset:4864\n"
    "v_mfma_f32_16x16x32_bf16 v[4:7], v[144:147], v[192:195], v[4:7]\n"
    "v_add_u32 v243, 16384, v243\n"
    "v_add_u32 v244, 16384, v244\n"
    "v_mfma_f32_16x16x32_bf16 v[8:11], v[144:147], v[208:211], v[8:11]\n"
    "buffer_load_dwordx4 v[108:111], v254, s[44:47], s27 offen offset:3072 \n"
    "v_mfma_f32_16x16x32_bf16 v[12:15], v[148:151], v[176:179], v[12:15]\n"
    "buffer_load_dwordx4 v[124:127], v254, s[44:47], s28 offen offset:3072 \n"
    "v_mfma_f32_16x16x32_bf16 v[16:19], v[148:151], v[192:195], v[16:19]\n"
    "s_waitcnt vmcnt(23)\n"
    "s_waitcnt lgkmcnt(0)\n"
    "s_barrier\n"
    "v_mfma_f32_16x16x32_bf16 v[20:23], v[148:151], v[208:211], v[20:23]\n"
    "ds_read_b64_tr_b16 v[144:145], v243 offset:8192\n"
    "v_mfma_f32_16x16x32_bf16 v[24:27], v[152:155], v[176:179], v[24:27]\n"
    "ds_read_b64_tr_b16 v[146:147], v243 offset:12288\n"
    "v_mfma_f32_16x16x32_bf16 v[28:31], v[152:155], v[192:195], v[28:31]\n"
    "ds_read_b64_tr_b16 v[148:149], v244 offset:8192\n"
    "v_mfma_f32_16x16x32_bf16 v[32:35], v[152:155], v[208:211], v[32:35]\n"
    "ds_read_b64_tr_b16 v[150:151], v244 offset:12288\n"
    "v_mfma_f32_16x16x32_bf16 v[36:39], v[156:159], v[176:179], v[36:39]\n"
    "ds_read_b64_tr_b16 v[152:153], v243 offset:8448\n"
    "v_mfma_f32_16x16x32_bf16 v[40:43], v[156:159], v[192:195], v[40:43]\n"
    "ds_read_b64_tr_b16 v[154:155], v243 offset:12544\n"
    "v_mfma_f32_16x16x32_bf16 v[44:47], v[156:159], v[208:211], v[44:47]\n"
    "ds_read_b64_tr_b16 v[156:157], v244 offset:8448\n"
    "v_mfma_f32_16x16x32_bf16 v[48:51], v[160:163], v[176:179], v[48:51]\n"
    "ds_read_b64_tr_b16 v[158:159], v244 offset:12544\n"
    "v_mfma_f32_16x16x32_bf16 v[52:55], v[160:163], v[192:195], v[52:55]\n"
    "buffer_load_dwordx4 v[140:143], v254, s[44:47], s29 offen offset:3072 \n"
    "v_mfma_f32_16x16x32_bf16 v[56:59], v[160:163], v[208:211], v[56:59]\n"
    "s_add_u32 m0, s68, 32768\n"
    "ds_read_b64_tr_b16 v[160:161], v243 offset:8704\n"
    "v_mfma_f32_16x16x32_bf16 v[60:63], v[164:167], v[176:179], v[60:63]\n"
    "ds_read_b64_tr_b16 v[162:163], v243 offset:12800\n"
    "v_mfma_f32_16x16x32_bf16 v[64:67], v[164:167], v[192:195], v[64:67]\n"
    "buffer_load_dwordx4 v250, s[40:43], s19 offen offset:0  lds\n"
    "v_mfma_f32_16x16x32_bf16 v[68:71], v[164:167], v[208:211], v[68:71]\n"
    "ds_read_b64_tr_b16 v[164:165], v244 offset:8704\n"
    "v_mfma_f32_16x16x32_bf16 v[72:75], v[168:171], v[176:179], v[72:75]\n"
    "ds_read_b64_tr_b16 v[166:167], v244 offset:12800\n"
    "v_mfma_f32_16x16x32_bf16 v[76:79], v[168:171], v[192:195], v[76:79]\n"
    "buffer_load_dwordx4 v251, s[40:43], s19 offen offset:1024  lds\n"
    "v_mfma_f32_16x16x32_bf16 v[80:83], v[168:171], v[208:211], v[80:83]\n"
    "ds_read_b64_tr_b16 v[168:169], v243 offset:8960\n"
    "v_mfma_f32_16x16x32_bf16 v[84:87], v[172:175], v[176:179], v[84:87]\n"
    "ds_read_b64_tr_b16 v[170:171], v243 offset:13056\n"
    "v_mfma_f32_16x16x32_bf16 v[88:91], v[172:175], v[192:195], v[88:91]\n"
    "buffer_load_dwordx4 v252, s[40:43], s19 offen offset:2048  lds\n"
    "v_mfma_f32_16x16x32_bf16 v[92:95], v[172:175], v[208:211], v[92:95]\n"
    "s_waitcnt lgkmcnt(6)\n"
    "ds_read_b64_tr_b16 v[172:173], v244 offset:8960\n"
    "v_mfma_f32_16x16x32_bf16 v[0:3], v[144:147], v[180:183], v[0:3]\n"
    "ds_read_b64_tr_b16 v[174:175], v244 offset:13056\n"
    "v_mfma_f32_16x16x32_bf16 v[4:7], v[144:147], v[196:199], v[4:7]\n"
    "buffer_load_dwordx4 v253, s[40:43], s19 offen offset:3072  lds\n"
    "v_mfma_f32_16x16x32_bf16 v[8:11], v[144:147], v[212:215], v[8:11]\n"
    "ds_read_b64_tr_b16 v[144:145], v243 offset:0\n"
    "v_mfma_f32_16x16x32_bf16 v[12:15], v[148:151], v[180:183], v[12:15]\n"
    "ds_read_b64_tr_b16 v[146:147], v243 offset:4096\n"
    "v_mfma_f32_16x16x32_bf16 v[16:19], v[148:151], v[196:199], v[16:19]\n"
    "buffer_load_dwordx4 v[176:179], v254, s[44:47], s30 offen offset:0 \n"
    "v_mfma_f32_16x16x32_bf16 v[20:23], v[148:151], v[212:215], v[20:23]\n"
    "ds_read_b64_tr_b16 v[148:149], v244 offset:0\n"
    "v_mfma_f32_16x16x32_bf16 v[24:27], v[152:155], v[180:183], v[24:27]\n"
    "ds_read_b64_tr_b16 v[150:151], v244 offset:4096\n"
    "v_mfma_f32_16x16x32_bf16 v[28:31], v[152:155], v[196:199], v[28:31]\n"
    "buffer_load_dwordx4 v[192:195], v254, s[44:47], s31 offen offset:0 \n"
    "v_mfma_f32_16x16x32_bf16 v[32:35], v[152:155], v[212:215], v[32:35]\n"
    "ds_read_b64_tr_b16 v[152:153], v243 offset:256\n"
    "v_mfma_f32_16x16x32_bf16 v[36:39], v[156:159], v[180:183], v[36:39]\n"
    "ds_read_b64_tr_b16 v[154:155], v243 offset:4352\n"
    "v_mfma_f32_16x16x32_bf16 v[40:43], v[156:159], v[196:199], v[40:43]\n"
    "buffer_load_dwordx4 v[208:211], v254, s[44:47], s32 offen offset:0 \n"
    "v_mfma_f32_16x16x32_bf16 v[44:47], v[156:159], v[212:215], v[44:47]\n"
    "s_waitcnt lgkmcnt(6)\n"
    "ds_read_b64_tr_b16 v[156:157], v244 offset:256\n"
    "v_mfma_f32_16x16x32_bf16 v[48:51], v[160:163], v[180:183], v[48:51]\n"
    "ds_read_b64_tr_b16 v[158:159], v244 offset:4352\n"
    "v_mfma_f32_16x16x32_bf16 v[52:55], v[160:163], v[196:199], v[52:55]\n"
    "v_mfma_f32_16x16x32_bf16 v[56:59], v[160:163], v[212:215], v[56:59]\n"
    "ds_read_b64_tr_b16 v[160:161], v243 offset:512\n"
    "v_mfma_f32_16x16x32_bf16 v[60:63], v[164:167], v[180:183], v[60:63]\n"
    "ds_read_b64_tr_b16 v[162:163], v243 offset:4608\n"
    "v_mfma_f32_16x16x32_bf16 v[64:67], v[164:167], v[196:199], v[64:67]\n"
    "s_waitcnt vmcnt(9)\n"
    "s_mov_b64 vcc, s[4:5]\n"
    "v_cndmask_b32_dpp v224, v100, v96, vcc quad_perm:[1,0,3,2] row_mask:0xf bank_mask:0xf\n"
    "v_cndmask_b32_dpp v232, v108, v104, vcc quad_perm:[1,0,3,2] row_mask:0xf bank_mask:0xf\n"
    "v_cndmask_b32_dpp v225, v101, v97, vcc quad_perm:[1,0,3,2] row_mask:0xf bank_mask:0xf\n"
    "v_mfma_f32_16x16x32_bf16 v[68:71], v[164:167], v[212:215], v[68:71]\n"
    "ds_read_b64_tr_b16 v[164:165], v244 offset:512\n"
    "v_cndmask_b32_dpp v233, v109, v105, vcc quad_perm:[1,0,3,2] row_mask:0xf bank_mask:0xf\n"
    "v_cndmask_b32_dpp v226, v102, v98, vcc quad_perm:[1,0,3,2] row_mask:0xf bank_mask:0xf\n"
    "v_cndmask_b32_dpp v234, v110, v106, vcc quad_perm:[1,0,3,2] row_mask:0xf bank_mask:0xf\n"
    "v_mfma_f32_16x16x32_bf16 v[72:75], v[168:171], v[180:183], v[72:75]\n"
    "ds_read_b64_tr_b16 v[166:167], v244 offset:4608\n"
    "v_cndmask_b32_dpp v227, v103, v99, vcc quad_perm:[1,0,3,2] row_mask:0xf bank_mask:0xf\n"
    "v_cndmask_b32_dpp v235, v111, v107, vcc quad_perm:[1,0,3,2] row_mask:0xf bank_mask:0xf\n"
    "v_mfma_f32_16x16x32_bf16 v[76:79], v[168:171], v[196:199], v[76:79]\n"
    "s_mov_b64 vcc, s[6:7]\n"
    "v_cndmask_b32_dpp v228, v96, v100, vcc quad_perm:[1,0,3,2] row_mask:0xf bank_mask:0xf\n"
    "v_cndmask_b32_dpp v236, v104, v108, vcc quad_perm:[1,0,3,2] row_mask:0xf bank_mask:0xf\n"
    "v_cndmask_b32_dpp v229, v97, v101, vcc quad_perm:[1,0,3,2] row_mask:0xf bank_mask:0xf\n"
    "v_cndmask_b32_dpp v237, v105, v109, vcc quad_perm:[1,0,3,2] row_mask:0xf bank_mask:0xf\n"
    "v_mfma_f32_16x16x32_bf16 v[80:83], v[168:171], v[212:215], v[80:83]\n"
    "ds_read_b64_tr_b16 v[168:169], v243 offset:768\n"
    "v_cndmask_b32_dpp v230, v98, v102, vcc quad_perm:[1,0,3,2] row_mask:0xf bank_mask:0xf\n"
    "v_cndmask_b32_dpp v238, v106, v110, vcc quad_perm:[1,0,3,2] row_mask:0xf bank_mask:0xf\n"
    "v_cndmask_b32_dpp v231, v99, v103, vcc quad_perm:[1,0,3,2] row_mask:0xf bank_mask:0xf\n"
    "v_mfma_f32_16x16x32_bf16 v[84:87], v[172:175], v[180:183], v[84:87]\n"
    "ds_read_b64_tr_b16 v[170:171], v243 offset:4864\n"
    "v_cndmask_b32_dpp v239, v107, v111, vcc quad_perm:[1,0,3,2] row_mask:0xf bank_mask:0xf\n"
    "v_mfma_f32_16x16x32_bf16 v[88:91], v[172:175], v[196:199], v[88:91]\n"
    "s_mov_b64 vcc, s[8:9]\n"
    "v_cndmask_b32_dpp v96, v232, v224, vcc quad_perm:[2,3,0,1] row_mask:0xf bank_mask:0xf\n"
    "v_cndmask_b32_dpp v100, v236, v228, vcc quad_perm:[2,3,0,1] row_mask:0xf bank_mask:0xf\n"
    "v_cndmask_b32_dpp v97, v233, v225, vcc quad_perm:[2,3,0,1] row_mask:0xf bank_mask:0xf\n"
    "v_cndmask_b32_dpp v101, v237, v229, vcc quad_perm:[2,3,0,1] row_mask:0xf bank_mask:0xf\n"
    "v_mfma_f32_16x16x32_bf16 v[92:95], v[172:175], v[212:215], v[92:95]\n"
    "s_waitcnt lgkmcnt(10)\n"
    "ds_read_b64_tr_b16 v[172:173], v244 offset:768\n"
    "v_cndmask_b32_dpp v98, v234, v226, vcc quad_perm:[2,3,0,1] row_mask:0xf bank_mask:0xf\n"
    "v_cndmask_b32_dpp v102, v238, v230, vcc quad_perm:[2,3,0,1] row_mask:0xf bank_mask:0xf\n"
    "v_mfma_f32_16x16x32_bf16 v[0:3], v[144:147], v[184:187], v[0:3]\n"
    "ds_read_b64_tr_b16 v[174:175], v244 offset:4864\n"
    "v_cndmask_b32_dpp v99, v235, v227, vcc quad_perm:[2,3,0,1] row_mask:0xf bank_mask:0xf\n"
    "v_cndmask_b32_dpp v103, v239, v231, vcc quad_perm:[2,3,0,1] row_mask:0xf bank_mask:0xf\n"
    "v_mfma_f32_16x16x32_bf16 v[4:7], v[144:147], v[200:203], v[4:7]\n"
    "s_mov_b64 vcc, s[10:11]\n"
    "v_add_u32 v243, 16384, v243\n"
    "v_add_u32 v244, 16384, v244\n"
    "v_cndmask_b32_dpp v104, v224, v232, vcc quad_perm:[2,3,0,1] row_mask:0xf bank_mask:0xf\n"
    "v_cndmask_b32_dpp v108, v228, v236, vcc quad_perm:[2,3,0,1] row_mask:0xf bank_mask:0xf\n"
    "v_mfma_f32_16x16x32_bf16 v[8:11], v[144:147], v[216:219], v[8:11]\n"
    "buffer_load_dwordx4 v[180:183], v254, s[44:47], s30 offen offset:1024 \n"
    "v_cndmask_b32_dpp v105, v225, v233, vcc quad_perm:[2,3,0,1] row_mask:0xf bank_mask:0xf\n"
    "v_cndmask_b32_dpp v109, v229, v237, vcc quad_perm:[2,3,0,1] row_mask:0xf bank_mask:0xf\n"
    "v_cndmask_b32_dpp v106, v226, v234, vcc quad_perm:[2,3,0,1] row_mask:0xf bank_mask:0xf\n"
    "v_mfma_f32_16x16x32_bf16 v[12:15], v[148:151], v[184:187], v[12:15]\n"
    "buffer_load_dwordx4 v[196:199], v254, s[44:47], s31 offen offset:1024 \n"
    "v_cndmask_b32_dpp v110, v230, v238, vcc quad_perm:[2,3,0,1] row_mask:0xf bank_mask:0xf\n"
    "v_cndmask_b32_dpp v107, v227, v235, vcc quad_perm:[2,3,0,1] row_mask:0xf bank_mask:0xf\n"
    "v_cndmask_b32_dpp v111, v231, v239, vcc quad_perm:[2,3,0,1] row_mask:0xf bank_mask:0xf\n"
    "v_mfma_f32_16x16x32_bf16 v[16:19], v[148:151], v[200:203], v[16:19]\n"
    "s_waitcnt lgkmcnt(0)\n"
    "s_barrier\n"
    "s_waitcnt vmcnt(10)\n"
    "s_mov_b64 vcc, s[4:5]\n"
    "v_mfma_f32_16x16x32_bf16 v[20:23], v[148:151], v[216:219], v[20:23]\n"
    "ds_read_b64_tr_b16 v[144:145], v243 offset:8192\n"
    "v_cndmask_b32_dpp v224, v116, v112, vcc quad_perm:[1,0,3,2] row_mask:0xf bank_mask:0xf\n"
    "v_cndmask_b32_dpp v232, v124, v120, vcc quad_perm:[1,0,3,2] row_mask:0xf bank_mask:0xf\n"
    "v_cndmask_b32_dpp v225, v117, v113, vcc quad_perm:[1,0,3,2] row_mask:0xf bank_mask:0xf\n"
    "v_mfma_f32_16x16x32_bf16 v[24:27], v[152:155], v[184:187], v[24:27]\n"
    "ds_read_b64_tr_b16 v[146:147], v243 offset:12288\n"
    "v_cndmask_b32_dpp v233, v125, v121, vcc quad_perm:[1,0,3,2] row_mask:0xf bank_mask:0xf\n"
    "v_cndmask_b32_dpp v226, v118, v114, vcc quad_perm:[1,0,3,2] row_mask:0xf bank_mask:0xf\n"
    "v_cndmask_b32_dpp v234, v126, v122, vcc quad_perm:[1,0,3,2] row_mask:0xf bank_mask:0xf\n"
    "v_mfma_f32_16x16x32_bf16 v[28:31], v[152:155], v[200:203], v[28:31]\n"
    "ds_read_b64_tr_b16 v[148:149], v244 offset:8192\n"
    "v_cndmask_b32_dpp v227, v119, v115, vcc quad_perm:[1,0,3,2] row_mask:0xf bank_mask:0xf\n"
    "v_cndmask_b32_dpp v235, v127, v123, vcc quad_perm:[1,0,3,2] row_mask:0xf bank_mask:0xf\n"
    "v_mfma_f32_16x16x32_bf16 v[32:35], v[152:155], v[216:219], v[32:35]\n"
    "s_mov_b64 vcc, s[6:7]\n"
    "ds_read_b64_tr_b16 v[150:151], v244 offset:12288\n"
    "v_cndmask_b32_dpp v228, v112, v116, vcc quad_perm:[1,0,3,2] row_mask:0xf bank_mask:0xf\n"
    "v_cndmask_b32_dpp v236, v120, v124, vcc quad_perm:[1,0,3,2] row_mask:0xf bank_mask:0xf\n"
    "v_mfma_f32_16x16x32_bf16 v[36:39], v[156:159], v[184:187], v[36:39]\n"
    "ds_read_b64_tr_b16 v[152:153], v243 offset:8448\n"
    "v_cndmask_b32_dpp v229, v113, v117, vcc quad_perm:[1,0,3,2] row_mask:0xf bank_mask:0xf\n"
    "v_cndmask_b32_dpp v237, v121, v125, vcc quad_perm:[1,0,3,2] row_mask:0xf bank_mask:0xf\n"
    "v_cndmask_b32_dpp v230, v114, v118, vcc quad_perm:[1,0,3,2] row_mask:0xf bank_mask:0xf\n"
    "v_mfma_f32_16x16x32_bf16 v[40:43], v[156:159], v[200:203], v[40:43]\n"
    "ds_read_b64_tr_b16 v[154:155], v243 offset:12544\n"
    "v_cndmask_b32_dpp v238, v122, v126, vcc quad_perm:[1,0,3,2] row_mask:0xf bank_mask:0xf\n"
    "v_cndmask_b32_dpp v231, v115, v119, vcc quad_perm:[1,0,3,2] row_mask:0xf bank_mask:0xf\n"
    "v_cndmask_b32_dpp v239, v123, v127, vcc quad_perm:[1,0,3,2] row_mask:0xf bank_mask:0xf\n"
    "v_mfma_f32_16x16x32_bf16 v[44:47], v[156:159], v[216:219], v[44:47]\n"
    "s_mov_b64 vcc, s[8:9]\n"
    "ds_read_b64_tr_b16 v[156:157], v244 offset:8448\n"
    "v_cndmask_b32_dpp v112, v232, v224, vcc quad_perm:[2,3,0,1] row_mask:0xf bank_mask:0xf\n"
    "v_cndmask_b32_dpp v116, v236, v228, vcc quad_perm:[2,3,0,1] row_mask:0xf bank_mask:0xf\n"
    "v_mfma_f32_16x16x32_bf16 v[48:51], v[160:163], v[184:187], v[48:51]\n"
    "ds_read_b64_tr_b16 v[158:159], v244 offset:12544\n"
    "v_cndmask_b32_dpp v113, v233, v225, vcc quad_perm:[2,3,0,1] row_mask:0xf bank_mask:0xf\n"
    "v_cndmask_b32_dpp v117, v237, v229, vcc quad_perm:[2,3,0,1] row_mask:0xf bank_mask:0xf\n"
    "v_cndmask_b32_dpp v114, v234, v226, vcc quad_perm:[2,3,0,1] row_mask:0xf bank_mask:0xf\n"
    "v_mfma_f32_16x16x32_bf16 v[52:55], v[160:163], v[200:203], v[52:55]\n"
    "buffer_load_dwordx4 v[212:215], v254, s[44:47], s32 offen offset:1024 \n"
    "v_cndmask_b32_dpp v118, v238, v230, vcc quad_perm:[2,3,0,1] row_mask:0xf bank_mask:0xf\n"
    "v_cndmask_b32_dpp v115, v235, v227, vcc quad_perm:[2,3,0,1] row_mask:0xf bank_mask:0xf\n"
    "v_cndmask_b32_dpp v119, v239, v231, vcc quad_perm:[2,3,0,1] row_mask:0xf bank_mask:0xf\n"
    "v_mfma_f32_16x16x32_bf16 v[56:59], v[160:163], v[216:219], v[56:59]\n"
    "s_mov_b64 vcc, s[10:11]\n"
    "ds_read_b64_tr_b16 v[160:161], v243 offset:8704\n"
    "v_cndmask_b32_dpp v120, v224, v232, vcc quad_perm:[2,3,0,1] row_mask:0xf bank_mask:0xf\n"
    "v_cndmask_b32_dpp v124, v228, v236, vcc quad_perm:[2,3,0,1] row_mask:0xf bank_mask:0xf\n"
    "v_mfma_f32_16x16x32_bf16 v[60:63], v[164:167], v[184:187], v[60:63]\n"
    "ds_read_b64_tr_b16 v[162:163], v243 offset:12800\n"
    "v_cndmask_b32_dpp v121, v225, v233, vcc quad_perm:[2,3,0,1] row_mask:0xf bank_mask:0xf\n"
    "v_cndmask_b32_dpp v125, v229, v237, vcc quad_perm:[2,3,0,1] row_mask:0xf bank_mask:0xf\n"
    "v_cndmask_b32_dpp v122, v226, v234, vcc quad_perm:[2,3,0,1] row_mask:0xf bank_mask:0xf\n"
    "v_mfma_f32_16x16x32_bf16 v[64:67], v[164:167], v[200:203], v[64:67]\n"
    "v_cndmask_b32_dpp v126, v230, v238, vcc quad_perm:[2,3,0,1] row_mask:0xf bank_mask:0xf\n"
    "v_cndmask_b32_dpp v123, v227, v235, vcc quad_perm:[2,3,0,1] row_mask:0xf bank_mask:0xf\n"
    "v_cndmask_b32_dpp v127, v231, v239, vcc quad_perm:[2,3,0,1] row_mask:0xf bank_mask:0xf\n"
    "v_mfma_f32_16x16x32_bf16 v[68:71], v[164:167], v[216:219], v[68:71]\n"
    "s_waitcnt vmcnt(10)\n"
    "s_mov_b64 vcc, s[4:5]\n"
    "ds_read_b64_tr_b16 v[164:165], v244 offset:8704\n"
    "v_cndmask_b32_dpp v224, v132, v128, vcc quad_perm:[1,0,3,2] row_mask:0xf bank_mask:0xf\n"
    "v_mfma_f32_16x16x32_bf16 v[72:75], v[168:171], v[184:187], v[72:75]\n"
    "ds_read_b64_tr_b16 v[166:167], v244 offset:12800\n"
    "v_cndmask_b32_dpp v232, v140, v136, vcc quad_perm:[1,0,3,2] row_mask:0xf bank_mask:0xf\n"
    "v_cndmask_b32_dpp v225, v133, v129, vcc quad_perm:[1,0,3,2] row_mask:0xf bank_mask:0xf\n"
    "v_cndmask_b32_dpp v233, v141, v137, vcc quad_perm:[1,0,3,2] row_mask:0xf bank_mask:0xf\n"
    "v_mfma_f32_16x16x32_bf16 v[76:79], v[168:171], v[200:203], v[76:79]\n"
    "v_cndmask_b32_dpp v226, v134, v130, vcc quad_perm:[1,0,3,2] row_mask:0xf bank_mask:0xf\n"
    "v_cndmask_b32_dpp v234, v142, v138, vcc quad_perm:[1,0,3,2] row_mask:0xf bank_mask:0xf\n"
    "v_cndmask_b32_dpp v227, v135, v131, vcc quad_perm:[1,0,3,2] row_mask:0xf bank_mask:0xf\n"
    "v_cndmask_b32_dpp v235, v143, v139, vcc quad_perm:[1,0,3,2] row_mask:0xf bank_mask:0xf\n"
    "v_mfma_f32_16x16x32_bf16 v[80:83], v[168:171], v[216:219], v[80:83]\n"
    "s_mov_b64 vcc, s[6:7]\n"
    "ds_read_b64_tr_b16 v[168:169], v243 offset:8960\n"
    "v_cndmask_b32_dpp v228, v128, v132, vcc quad_perm:[1,0,3,2] row_mask:0xf bank_mask:0xf\n"
    "v_cndmask_b32_dpp v236, v136, v140, vcc quad_perm:[1,0,3,2] row_mask:0xf bank_mask:0xf\n"
    "v_mfma_f32_16x16x32_bf16 v[84:87], v[172:175], v[184:187], v[84:87]\n"
    "ds_read_b64_tr_b16 v[170:171], v243 offset:13056\n"
    "v_cndmask_b32_dpp v229, v129, v133, vcc quad_perm:[1,0,3,2] row_mask:0xf bank_mask:0xf\n"
    "v_cndmask_b32_dpp v237, v137, v141, vcc quad_perm:[1,0,3,2] row_mask:0xf bank_mask:0xf\n"
    "v_cndmask_b32_dpp v230, v130, v134, vcc quad_perm:[1,0,3,2] row_mask:0xf bank_mask:0xf\n"
    "v_mfma_f32_16x16x32_bf16 v[88:91], v[172:175], v[200:203], v[88:91]\n"
    "v_cndmask_b32_dpp v238, v138, v142, vcc quad_perm:[1,0,3,2] row_mask:0xf bank_mask:0xf\n"
    "v_cndmask_b32_dpp v231, v131, v135, vcc quad_perm:[1,0,3,2] row_mask:0xf bank_mask:0xf\n"
    "v_cndmask_b32_dpp v239, v139, v143, vcc quad_perm:[1,0,3,2] row_mask:0xf bank_mask:0xf\n"
    "v_mfma_f32_16x16x32_bf16 v[92:95], v[172:175], v[216:219], v[92:95]\n"
    "s_waitcnt lgkmcnt(6)\n"
    "s_mov_b64 vcc, s[8:9]\n"
    "ds_read_b64_tr_b16 v[172:173], v244 offset:8960\n"
    "v_cndmask_b32_dpp v128, v232, v224, vcc quad_perm:[2,3,0,1] row_mask:0xf bank_mask:0xf\n"
    "v_mfma_f32_16x16x32_bf16 v[0:3], v[144:147], v[188:191], v[0:3]\n"
    "ds_read_b64_tr_b16 v[174:175], v244 offset:13056\n"
    "v_cndmask_b32_dpp v132, v236, v228, vcc quad_perm:[2,3,0,1] row_mask:0xf bank_mask:0xf\n"
    "v_cndmask_b32_dpp v129, v233, v225, vcc quad_perm:[2,3,0,1] row_mask:0xf bank_mask:0xf\n"
    "v_cndmask_b32_dpp v133, v237, v229, vcc quad_perm:[2,3,0,1] row_mask:0xf bank_mask:0xf\n"
    "v_mfma_f32_16x16x32_bf16 v[4:7], v[144:147], v[204:207], v[4:7]\n"
    "buffer_load_dwordx4 v[184:187], v254, s[44:47], s30 offen offset:2048 \n"
    "v_cndmask_b32_dpp v130, v234, v226, vcc quad_perm:[2,3,0,1] row_mask:0xf bank_mask:0xf\n"
    "v_cndmask_b32_dpp v134, v238, v230, vcc quad_perm:[2,3,0,1] row_mask:0xf bank_mask:0xf\n"
    "v_cndmask_b32_dpp v131, v235, v227, vcc quad_perm:[2,3,0,1] row_mask:0xf bank_mask:0xf\n"
    "v_mfma_f32_16x16x32_bf16 v[8:11], v[144:147], v[220:223], v[8:11]\n"
    "ds_read_b64_tr_b16 v[144:145], v243 offset:0\n"
    "v_cndmask_b32_dpp v135, v239, v231, vcc quad_perm:[2,3,0,1] row_mask:0xf bank_mask:0xf\n"
    "v_mfma_f32_16x16x32_bf16 v[12:15], v[148:151], v[188:191], v[12:15]\n"
    "s_mov_b64 vcc, s[10:11]\n"
    "ds_read_b64_tr_b16 v[146:147], v243 offset:4096\n"
    "v_cndmask_b32_dpp v136, v224, v232, vcc quad_perm:[2,3,0,1] row_mask:0xf bank_mask:0xf\n"
    "v_cndmask_b32_dpp v140, v228, v236, vcc quad_perm:[2,3,0,1] row_mask:0xf bank_mask:0xf\n"
    "v_mfma_f32_16x16x32_bf16 v[16:19], v[148:151], v[204:207], v[16:19]\n"
    "buffer_load_dwordx4 v[200:203], v254, s[44:47], s31 offen offset:2048 \n"
    "v_cndmask_b32_dpp v137, v225, v233, vcc quad_perm:[2,3,0,1] row_mask:0xf bank_mask:0xf\n"
    "v_cndmask_b32_dpp v141, v229, v237, vcc quad_perm:[2,3,0,1] row_mask:0xf bank_mask:0xf\n"
    "v_cndmask_b32_dpp v138, v226, v234, vcc quad_perm:[2,3,0,1] row_mask:0xf bank_mask:0xf\n"
    "v_mfma_f32_16x16x32_bf16 v[20:23], v[148:151], v[220:223], v[20:23]\n"
    "ds_read_b64_tr_b16 v[148:149], v244 offset:0\n"
    "v_cndmask_b32_dpp v142, v230, v238, vcc quad_perm:[2,3,0,1] row_mask:0xf bank_mask:0xf\n"
    "v_cndmask_b32_dpp v139, v227, v235, vcc quad_perm:[2,3,0,1] row_mask:0xf bank_mask:0xf\n"
    "v_cndmask_b32_dpp v143, v231, v239, vcc quad_perm:[2,3,0,1] row_mask:0xf bank_mask:0xf\n"
    "v_mfma_f32_16x16x32_bf16 v[24:27], v[152:155], v[188:191], v[24:27]\n"
    "ds_read_b64_tr_b16 v[150:151], v244 offset:4096\n"
    "v_mfma_f32_16x16x32_bf16 v[28:31], v[152:155], v[204:207], v[28:31]\n"
    "buffer_load_dwordx4 v[216:219], v254, s[44:47], s32 offen offset:2048 \n"
    "v_mfma_f32_16x16x32_bf16 v[32:35], v[152:155], v[220:223], v[32:35]\n"
    "ds_read_b64_tr_b16 v[152:153], v243 offset:256\n"
    "v_mfma_f32_16x16x32_bf16 v[36:39], v[156:159], v[188:191], v[36:39]\n"
    "ds_read_b64_tr_b16 v[154:155], v243 offset:4352\n"
    "v_mfma_f32_16x16x32_bf16 v[40:43], v[156:159], v[204:207], v[40:43]\n"
    "v_mfma_f32_16x16x32_bf16 v[44:47], v[156:159], v[220:223], v[44:47]\n"
    "s_waitcnt lgkmcnt(6)\n"
    "ds_read_b64_tr_b16 v[156:157], v244 offset:256\n"
    "v_mfma_f32_16x16x32_bf16 v[48:51], v[160:163], v[188:191], v[48:51]\n"
    "ds_read_b64_tr_b16 v[158:159], v244 offset:4352\n"
    "v_mfma_f32_16x16x32_bf16 v[52:55], v[160:163], v[204:207], v[52:55]\n"
    "v_mfma_f32_16x16x32_bf16 v[56:59], v[160:163], v[220:223], v[56:59]\n"
    "ds_read_b64_tr_b16 v[160:161], v243 offset:512\n"
    "v_mfma_f32_16x16x32_bf16 v[60:63], v[164:167], v[188:191], v[60:63]\n"
    "ds_read_b64_tr_b16 v[162:163], v243 offset:4608\n"
    "v_mfma_f32_16x16x32_bf16 v[64:67], v[164:167], v[204:207], v[64:67]\n"
    "v_mfma_f32_16x16x32_bf16 v[68:71], v[164:167], v[220:223], v[68:71]\n"
    "ds_read_b64_tr_b16 v[164:165], v244 offset:512\n"
    "v_mfma_f32_16x16x32_bf16 v[72:75], v[168:171], v[188:191], v[72:75]\n"
    "ds_read_b64_tr_b16 v[166:167], v244 offset:4608\n"
    "v_mfma_f32_16x16x32_bf16 v[76:79], v[168:171], v[204:207], v[76:79]\n"
    "v_mfma_f32_16x16x32_bf16 v[80:83], v[168:171], v[220:223], v[80:83]\n"
    "ds_read_b64_tr_b16 v[168:169], v243 offset:768\n"
    "v_mfma_f32_16x16x32_bf16 v[84:87], v[172:175], v[188:191], v[84:87]\n"
    "ds_read_b64_tr_b16 v[170:171], v243 offset:4864\n"
    "v_mfma_f32_16x16x32_bf16 v[88:91], v[172:175], v[204:207], v[88:91]\n"
    "v_mfma_f32_16x16x32_bf16 v[92:95], v[172:175], v[220:223], v[92:95]\n"
    "s_waitcnt lgkmcnt(10)\n"
    "ds_read_b64_tr_b16 v[172:173], v244 offset:768\n"
    "v_mfma_f32_16x16x32_bf16 v[0:3], v[144:147], v[96:99], v[0:3]\n"
    "ds_read_b64_tr_b16 v[174:175], v244 offset:4864\n"
    "v_mfma_f32_16x16x32_bf16 v[4:7], v[144:147], v[112:115], v[4:7]\n"
    "v_add_u32 v243, 0xffff0000, v243\n"
    "v_add_u32 v244, 0xffff0000, v244\n"
    "v_mfma_f32_16x16x32_bf16 v[8:11], v[144:147], v[128:131], v[8:11]\n"
    "buffer_load_dwordx4 v[188:191], v254, s[44:47], s30 offen offset:3072 \n"
    "v_mfma_f32_16x16x32_bf16 v[12:15], v[148:151], v[96:99], v[12:15]\n"
    "buffer_load_dwordx4 v[204:207], v254, s[44:47], s31 offen offset:3072 \n"
    "v_mfma_f32_16x16x32_bf16 v[16:19], v[148:151], v[112:115], v[16:19]\n"
    "s_waitcnt lgkmcnt(0)\n"
    "s_barrier\n"
    "v_mfma_f32_16x16x32_bf16 v[20:23], v[148:151], v[128:131], v[20:23]\n"
    "ds_read_b64_tr_b16 v[144:145], v243 offset:8192\n"
    "v_mfma_f32_16x16x32_bf16 v[24:27], v[152:155], v[96:99], v[24:27]\n"
    "ds_read_b64_tr_b16 v[146:147], v243 offset:12288\n"
    "v_mfma_f32_16x16x32_bf16 v[28:31], v[152:155], v[112:115], v[28:31]\n"
    "ds_read_b64_tr_b16 v[148:149], v244 offset:8192\n"
    "v_mfma_f32_16x16x32_bf16 v[32:35], v[152:155], v[128:131], v[32:35]\n"
    "ds_read_b64_tr_b16 v[150:151], v244 offset:12288\n"
    "v_mfma_f32_16x16x32_bf16 v[36:39], v[156:159], v[96:99], v[36:39]\n"
    "ds_read_b64_tr_b16 v[152:153], v243 offset:8448\n"
    "v_mfma_f32_16x16x32_bf16 v[40:43], v[156:159], v[112:115], v[40:43]\n"
    "ds_read_b64_tr_b16 v[154:155], v243 offset:12544\n"
    "v_mfma_f32_16x16x32_bf16 v[44:47], v[156:159], v[128:131], v[44:47]\n"
    "ds_read_b64_tr_b16 v[156:157], v244 offset:8448\n"
    "v_mfma_f32_16x16x32_bf16 v[48:51], v[160:163], v[96:99], v[48:51]\n"
    "ds_read_b64_tr_b16 v[158:159], v244 offset:12544\n"
    "v_mfma_f32_16x16x32_bf16 v[52:55], v[160:163], v[112:115], v[52:55]\n"
    "buffer_load_dwordx4 v[220:223], v254, s[44:47], s32 offen offset:3072 \n"
    "v_mfma_f32_16x16x32_bf16 v[56:59], v[160:163], v[128:131], v[56:59]\n"
    "s_add_u32 m0, s68, 49152\n"
    "ds_read_b64_tr_b16 v[160:161], v243 offset:8704\n"
    "v_mfma_f32_16x16x32_bf16 v[60:63], v[164:167], v[96:99], v[60:63]\n"
    "ds_read_b64_tr_b16 v[162:163], v243 offset:12800\n"
    "v_mfma_f32_16x16x32_bf16 v[64:67], v[164:167], v[112:115], v[64:67]\n"
    "s_cmp_eq_u32 s69, 0\n"
    "s_cbranch_scc1 Lk0%=\n"
    "buffer_load_dwordx4 v250, s[40:43], s20 offen offset:0  lds\n"
    "buffer_load_dwordx4 v251, s[40:43], s20 offen offset:1024  lds\n"
    "buffer_load_dwordx4 v252, s[40:43], s20 offen offset:2048  lds\n"
    "buffer_load_dwordx4 v253, s[40:43], s20 offen offset:3072  lds\n"
    "s_branch Lk1%=\n"
    "Lk0%=:\n"
    "buffer_load_dwordx4 v246, s[36:39], s70 offen offset:0 nt lds\n"
    "buffer_load_dwordx4 v247, s[36:39], s70 offen offset:1024 nt lds\n"
    "buffer_load_dwordx4 v246, s[36:39], s70 offen offset:2048 nt lds\n"
    "buffer_load_dwordx4 v247, s[36:39], s70 offen offset:3072 nt lds\n"
    "s_add_u32 s70, s70, 0x2000\n"
    "Lk1%=:\n"
    "v_mfma_f32_16x16x32_bf16 v[68:71], v[164:167], v[128:131], v[68:71]\n"
    "s_add_u32 m0, s68, 65536\n"
    "s_cmp_lt_u32 s73, 0x10000000\n"
    "s_cselect_b32 s35, s70, 0x40000000\n"
    "s_cselect_b32 s34, 0x2000, 0\n"
    "ds_read_b64_tr_b16 v[164:165], v244 offset:8704\n"
    "v_mfma_f32_16x16x32_bf16 v[72:75], v[168:171], v[96:99], v[72:75]\n"
    "ds_read_b64_tr_b16 v[166:167], v244 offset:12800\n"
    "v_mfma_f32_16x16x32_bf16 v[76:79], v[168:171], v[112:115], v[76:79]\n"
    "buffer_load_dwordx4 v246, s[36:39], s35 offen offset:0 nt lds\n"
    "v_mfma_f32_16x16x32_bf16 v[80:83], v[168:171], v[128:131], v[80:83]\n"
    "ds_read_b64_tr_b16 v[168:169], v243 offset:8960\n"
    "v_mfma_f32_16x16x32_bf16 v[84:87], v[172:175], v[96:99], v[84:87]\n"
    "ds_read_b64_tr_b16 v[170:171], v243 offset:13056\n"
    "v_mfma_f32_16x16x32_bf16 v[88:91], v[172:175], v[112:115], v[88:91]\n"
    "buffer_load_dwordx4 v247, s[36:39], s35 offen offset:1024 nt lds\n"
    "v_mfma_f32_16x16x32_bf16 v[92:95], v[172:175], v[128:131], v[92:95]\n"
    "s_waitcnt lgkmcnt(6)\n"
    "ds_read_b64_tr_b16 v[172:173], v244 offset:8960\n"
    "v_mfma_f32_16x16x32_bf16 v[0:3], v[144:147], v[100:103], v[0:3]\n"
    "ds_read_b64_tr_b16 v[174:175], v244 offset:13056\n"
    "v_mfma_f32_16x16x32_bf16 v[4:7], v[144:147], v[116:119], v[4:7]\n"
    "buffer_load_dwordx4 v246, s[36:39], s35 offen offset:2048 nt lds\n"
    "v_mfma_f32_16x16x32_bf16 v[8:11], v[144:147], v[132:135], v[8:11]\n"
    "ds_read_b64_tr_b16 v[144:145], v243 offset:0\n"
    "v_mfma_f32_16x16x32_bf16 v[12:15], v[148:151], v[100:103], v[12:15]\n"
    "ds_read_b64_tr_b16 v[146:147], v243 offset:4096\n"
    "v_mfma_f32_16x16x32_bf16 v[16:19], v[148:151], v[116:119], v[16:19]\n"
    "buffer_load_dwordx4 v247, s[36:39], s35 offen offset:3072 nt lds\n"
    "v_mfma_f32_16x16x32_bf16 v[20:23], v[148:151], v[132:135], v[20:23]\n"
    "s_add_u32 s70, s70, s34\n"
    "ds_read_b64_tr_b16 v[148:149], v244 offset:0\n"
    "v_mfma_f32_16x16x32_bf16 v[24:27], v[152:155], v[100:103], v[24:27]\n"
    "ds_read_b64_tr_b16 v[150:151], v244 offset:4096\n"
    "v_mfma_f32_16x16x32_bf16 v[28:31], v[152:155], v[116:119], v[28:31]\n"
    "v_mfma_f32_16x16x32_bf16 v[32:35], v[152:155], v[132:135], v[32:35]\n"
    "ds_read_b64_tr_b16 v[152:153], v243 offset:256\n"
    "v_mfma_f32_16x16x32_bf16 v[36:39], v[156:159], v[100:103], v[36:39]\n"
    "ds_read_b64_tr_b16 v[154:155], v243 offset:4352\n"
    "v_mfma_f32_16x16x32_bf16 v[40:43], v[156:159], v[116:119], v[40:43]\n"
    "v_mfma_f32_16x16x32_bf16 v[44:47], v[156:159], v[132:135], v[44:47]\n"
    "s_waitcnt lgkmcnt(6)\n"
    "ds_read_b64_tr_b16 v[156:157], v244 offset:256\n"
    "v_mfma_f32_16x16x32_bf16 v[48:51], v[160:163], v[100:103], v[48:51]\n"
    "ds_read_b64_tr_b16 v[158:159], v244 offset:4352\n"
    "v_mfma_f32_16x16x32_bf16 v[52:55], v[160:163], v[116:119], v[52:55]\n"
    "v_mfma_f32_16x16x32_bf16 v[56:59], v[160:163], v[132:135], v[56:59]\n"
    "ds_read_b64_tr_b16 v[160:161], v243 offset:512\n"
    "v_mfma_f32_16x16x32_bf16 v[60:63], v[164:167], v[100:103], v[60:63]\n"
    "ds_read_b64_tr_b16 v[162:163], v243 offset:4608\n"
    "v_mfma_f32_16x16x32_bf16 v[64:67], v[164:167], v[116:119], v[64:67]\n"
    "s_waitcnt vmcnt(10)\n"
    "s_mov_b64 vcc, s[4:5]\n"
    "v_cndmask_b32_dpp v224, v180, v176, vcc quad_perm:[1,0,3,2] row_mask:0xf bank_mask:0xf\n"
    "v_cndmask_b32_dpp v232, v188, v184, vcc quad_perm:[1,0,3,2] row_mask:0xf bank_mask:0xf\n"
    "v_cndmask_b32_dpp v225, v181, v177, vcc quad_perm:[1,0,3,2] row_mask:0xf bank_mask:0xf\n"
    "v_mfma_f32_16x16x32_bf16 v[68:71], v[164:167], v[132:135], v[68:71]\n"
    "ds_read_b64_tr_b16 v[164:165], v244 offset:512\n"
    "v_cndmask_b32_dpp v233, v189, v185, vcc quad_perm:[1,0,3,2] row_mask:0xf bank_mask:0xf\n"
    "v_cndmask_b32_dpp v226, v182, v178, vcc quad_perm:[1,0,3,2] row_mask:0xf bank_mask:0xf\n"
    "v_cndmask_b32_dpp v234, v190, v186, vcc quad_perm:[1,0,3,2] row_mask:0xf bank_mask:0xf\n"
    "v_mfma_f32_16x16x32_bf16 v[72:75], v[168:171], v[100:103], v[72:75]\n"
    "ds_read_b64_tr_b16 v[166:167], v244 offset:4608\n"
    "v_cndmask_b32_dpp v227, v183, v179, vcc quad_perm:[1,0,3,2] row_mask:0xf bank_mask:0xf\n"
    "v_cndmask_b32_dpp v235, v191, v187, vcc quad_perm:[1,0,3,2] row_mask:0xf bank_mask:0xf\n"
    "v_mfma_f32_16x16x32_bf16 v[76:79], v[168:171], v[116:119], v[76:79]\n"
    "s_mov_b64 vcc, s[6:7]\n"
    "v_cndmask_b32_dpp v228, v176, v180, vcc quad_perm:[1,0,3,2] row_mask:0xf bank_mask:0xf\n"
    "v_cndmask_b32_dpp v236, v184, v188, vcc quad_perm:[1,0,3,2] row_mask:0xf bank_mask:0xf\n"
    "v_cndmask_b32_dpp v229, v177, v181, vcc quad_perm:[1,0,3,2] row_mask:0xf bank_mask:0xf\n"
    "v_cndmask_b32_dpp v237, v185, v189, vcc quad_perm:[1,0,3,2] row_mask:0xf bank_mask:0xf\n"
    "v_mfma_f32_16x16x32_bf16 v[80:83], v[168:171], v[132:135], v[80:83]\n"
    "ds_read_b64_tr_b16 v[168:169], v243 offset:768\n"
    "v_cndmask_b32_dpp v230, v178, v182, vcc quad_perm:[1,0,3,2] row_mask:0xf bank_mask:0xf\n"
    "v_cndmask_b32_dpp v238, v186, v190, vcc quad_perm:[1,0,3,2] row_mask:0xf bank_mask:0xf\n"
    "v_cndmask_b32_dpp v231, v179, v183, vcc quad_perm:[1,0,3,2] row_mask:0xf bank_mask:0xf\n"
    "v_mfma_f32_16x16x32_bf16 v[84:87], v[172:175], v[100:103], v[84:87]\n"
    "ds_read_b64_tr_b16 v[170:171], v243 offset:4864\n"
    "v_cndmask_b32_dpp v239, v187, v191, vcc quad_perm:[1,0,3,2] row_mask:0xf bank_mask:0xf\n"
    "v_mfma_f32_16x16x32_bf16 v[88:91], v[172:175], v[116:119], v[88:91]\n"
    "s_mov_b64 vcc, s[8:9]\n"
    "v_cndmask_b32_dpp v176, v232, v224, vcc quad_perm:[2,3,0,1] row_mask:0xf bank_mask:0xf\n"
    "v_cndmask_b32_dpp v180, v236, v228, vcc quad_perm:[2,3,0,1] row_mask:0xf bank_mask:0xf\n"
    "v_cndmask_b32_dpp v177, v233, v225, vcc quad_perm:[2,3,0,1] row_mask:0xf bank_mask:0xf\n"
    "v_cndmask_b32_dpp v181, v237, v229, vcc quad_perm:[2,3,0,1] row_mask:0xf bank_mask:0xf\n"
    "v_mfma_f32_16x16x32_bf16 v[92:95], v[172:175], v[132:135], v[92:95]\n"
    "s_waitcnt lgkmcnt(10)\n"
    "ds_read_b64_tr_b16 v[172:173], v244 offset:768\n"
    "v_cndmask_b32_dpp v178, v234, v226, vcc quad_perm:[2,3,0,1] row_mask:0xf bank_mask:0xf\n"
    "v_cndmask_b32_dpp v182, v238, v230, vcc quad_perm:[2,3,0,1] row_mask:0xf bank_mask:0xf\n"
    "v_mfma_f32_16x16x32_bf16 v[0:3], v[144:147], v[104:107], v[0:3]\n"
    "ds_read_b64_tr_b16 v[174:175], v244 offset:4864\n"
    "v_cndmask_b32_dpp v179, v235, v227, vcc quad_perm:[2,3,0,1] row_mask:0xf bank_mask:0xf\n"
    "v_cndmask_b32_dpp v183, v239, v231, vcc quad_perm:[2,3,0,1] row_mask:0xf bank_mask:0xf\n"
    "v_mfma_f32_16x16x32_bf16 v[4:7], v[144:147], v[120:123], v[4:7]\n"
    "s_mov_b64 vcc, s[10:11]\n"
    "v_add_u32 v243, 16384, v243\n"
    "v_add_u32 v244, 16384, v244\n"
    "v_cndmask_b32_dpp v184, v224, v232, vcc quad_perm:[2,3,0,1] row_mask:0xf bank_mask:0xf\n"
    "v_cndmask_b32_dpp v188, v228, v236, vcc quad_perm:[2,3,0,1] row_mask:0xf bank_mask:0xf\n"
    "v_mfma_f32_16x16x32_bf16 v[8:11], v[144:147], v[136:139], v[8:11]\n"
    "v_cndmask_b32_dpp v185, v225, v233, vcc quad_perm:[2,3,0,1] row_mask:0xf bank_mask:0xf\n"
    "v_cndmask_b32_dpp v189, v229, v237, vcc quad_perm:[2,3,0,1] row_mask:0xf bank_mask:0xf\n"
    "v_cndmask_b32_dpp v186, v226, v234, vcc quad_perm:[2,3,0,1] row_mask:0xf bank_mask:0xf\n"
    "v_cndmask_b32_dpp v190, v230, v238, vcc quad_perm:[2,3,0,1] row_mask:0xf bank_mask:0xf\n"
    "v_mfma_f32_16x16x32_bf16 v[12:15], v[148:151], v[104:107], v[12:15]\n"
    "v_cndmask_b32_dpp v187, v227, v235, vcc quad_perm:[2,3,0,1] row_mask:0xf bank_mask:0xf\n"
    "v_cndmask_b32_dpp v191, v231, v239, vcc quad_perm:[2,3,0,1] row_mask:0xf bank_mask:0xf\n"
    "v_mfma_f32_16x16x32_bf16 v[16:19], v[148:151], v[120:123], v[16:19]\n"
    "s_waitcnt lgkmcnt(0)\n"
    "s_barrier\n"
    "s_waitcnt vmcnt(9)\n"
    "s_mov_b64 vcc, s[4:5]\n"
    "v_mfma_f32_16x16x32_bf16 v[20:23], v[148:151], v[136:139], v[20:23]\n"
    "ds_read_b64_tr_b16 v[144:145], v243 offset:8192\n"
    "v_cndmask_b32_dpp v224, v196, v192, vcc quad_perm:[1,0,3,2] row_mask:0xf bank_mask:0xf\n"
    "v_cndmask_b32_dpp v232, v204, v200, vcc quad_perm:[1,0,3,2] row_mask:0xf bank_mask:0xf\n"
    "v_cndmask_b32_dpp v225, v197, v193, vcc quad_perm:[1,0,3,2] row_mask:0xf bank_mask:0xf\n"
    "v_mfma_f32_16x16x32_bf16 v[24:27], v[152:155], v[104:107], v[24:27]\n"
    "s_add_u32 m0, s68, 0\n"
    "ds_read_b64_tr_b16 v[146:147], v243 offset:12288\n"
    "v_cndmask_b32_dpp v233, v205, v201, vcc quad_perm:[1,0,3,2] row_mask:0xf bank_mask:0xf\n"
    "v_cndmask_b32_dpp v226, v198, v194, vcc quad_perm:[1,0,3,2] row_mask:0xf bank_mask:0xf\n"
    "v_mfma_f32_16x16x32_bf16 v[28:31], v[152:155], v[120:123], v[28:31]\n"
    "ds_read_b64_tr_b16 v[148:149], v244 offset:8192\n"
    "v_cndmask_b32_dpp v234, v206, v202, vcc quad_perm:[1,0,3,2] row_mask:0xf bank_mask:0xf\n"
    "v_cndmask_b32_dpp v227, v199, v195, vcc quad_perm:[1,0,3,2] row_mask:0xf bank_mask:0xf\n"
    "v_cndmask_b32_dpp v235, v207, v203, vcc quad_perm:[1,0,3,2] row_mask:0xf bank_mask:0xf\n"
    "v_mfma_f32_16x16x32_bf16 v[32:35], v[152:155], v[136:139], v[32:35]\n"
    "s_mov_b64 vcc, s[6:7]\n"
    "ds_read_b64_tr_b16 v[150:151], v244 offset:12288\n"
    "v_cndmask_b32_dpp v228, v192, v196, vcc quad_perm:[1,0,3,2] row_mask:0xf bank_mask:0xf\n"
    "v_cndmask_b32_dpp v236, v200, v204, vcc quad_perm:[1,0,3,2] row_mask:0xf bank_mask:0xf\n"
    "v_mfma_f32_16x16x32_bf16 v[36:39], v[156:159], v[104:107], v[36:39]\n"
    "ds_read_b64_tr_b16 v[152:153], v243 offset:8448\n"
    "v_cndmask_b32_dpp v229, v193, v197, vcc quad_perm:[1,0,3,2] row_mask:0xf bank_mask:0xf\n"
    "v_cndmask_b32_dpp v237, v201, v205, vcc quad_perm:[1,0,3,2] row_mask:0xf bank_mask:0xf\n"
    "v_cndmask_b32_dpp v230, v194, v198, vcc quad_perm:[1,0,3,2] row_mask:0xf bank_mask:0xf\n"
    "v_mfma_f32_16x16x32_bf16 v[40:43], v[156:159], v[120:123], v[40:43]\n"
    "ds_read_b64_tr_b16 v[154:155], v243 offset:12544\n"
    "v_cndmask_b32_dpp v238, v202, v206, vcc quad_perm:[1,0,3,2] row_mask:0xf bank_mask:0xf\n"
    "v_cndmask_b32_dpp v231, v195, v199, vcc quad_perm:[1,0,3,2] row_mask:0xf bank_mask:0xf\n"
    "v_cndmask_b32_dpp v239, v203, v207, vcc quad_perm:[1,0,3,2] row_mask:0xf bank_mask:0xf\n"
    "v_mfma_f32_16x16x32_bf16 v[44:47], v[156:159], v[136:139], v[44:47]\n"
    "s_mov_b64 vcc, s[8:9]\n"
    "ds_read_b64_tr_b16 v[156:157], v244 offset:8448\n"
    "v_cndmask_b32_dpp v192, v232, v224, vcc quad_perm:[2,3,0,1] row_mask:0xf bank_mask:0xf\n"
    "v_cndmask_b32_dpp v196, v236, v228, vcc quad_perm:[2,3,0,1] row_mask:0xf bank_mask:0xf\n"
    "v_mfma_f32_16x16x32_bf16 v[48:51], v[160:163], v[104:107], v[48:51]\n"
    "ds_read_b64_tr_b16 v[158:159], v244 offset:12544\n"
    "v_cndmask_b32_dpp v193, v233, v225, vcc quad_perm:[2,3,0,1] row_mask:0xf bank_mask:0xf\n"
    "v_cndmask_b32_dpp v197, v237, v229, vcc quad_perm:[2,3,0,1] row_mask:0xf bank_mask:0xf\n"
    "v_cndmask_b32_dpp v194, v234, v226, vcc quad_perm:[2,3,0,1] row_mask:0xf bank_mask:0xf\n"
    "v_mfma_f32_16x16x32_bf16 v[52:55], v[160:163], v[120:123], v[52:55]\n"
    "buffer_load_dwordx4 v246, s[36:39], s70 offen offset:0 nt lds\n"
    "v_cndmask_b32_dpp v198, v238, v230, vcc quad_perm:[2,3,0,1] row_mask:0xf bank_mask:0xf\n"
    "v_cndmask_b32_dpp v195, v235, v227, vcc quad_perm:[2,3,0,1] row_mask:0xf bank_mask:0xf\n"
    "v_cndmask_b32_dpp v199, v239, v231, vcc quad_perm:[2,3,0,1] row_mask:0xf bank_mask:0xf\n"
    "v_mfma_f32_16x16x32_bf16 v[56:59], v[160:163], v[136:139], v[56:59]\n"
    "s_mov_b64 vcc, s[10:11]\n"
    "ds_read_b64_tr_b16 v[160:161], v243 offset:8704\n"
    "v_cndmask_b32_dpp v200, v224, v232, vcc quad_perm:[2,3,0,1] row_mask:0xf bank_mask:0xf\n"
    "v_cndmask_b32_dpp v204, v228, v236, vcc quad_perm:[2,3,0,1] row_mask:0xf bank_mask:0xf\n"
    "v_mfma_f32_16x16x32_bf16 v[60:63], v[164:167], v[104:107], v[60:63]\n"
    "ds_read_b64_tr_b16 v[162:163], v243 offset:12800\n"
    "v_cndmask_b32_dpp v201, v225, v233, vcc quad_perm:[2,3,0,1] row_mask:0xf bank_mask:0xf\n"
    "v_cndmask_b32_dpp v205, v229, v237, vcc quad_perm:[2,3,0,1] row_mask:0xf bank_mask:0xf\n"
    "v_cndmask_b32_dpp v202, v226, v234, vcc quad_perm:[2,3,0,1] row_mask:0xf bank_mask:0xf\n"
    "v_mfma_f32_16x16x32_bf16 v[64:67], v[164:167], v[120:123], v[64:67]\n"
    "buffer_load_dwordx4 v247, s[36:39], s70 offen offset:1024 nt lds\n"
    "v_cndmask_b32_dpp v206, v230, v238, vcc quad_perm:[2,3,0,1] row_mask:0xf bank_mask:0xf\n"
    "v_cndmask_b32_dpp v203, v227, v235, vcc quad_perm:[2,3,0,1] row_mask:0xf bank_mask:0xf\n"
    "v_cndmask_b32_dpp v207, v231, v239, vcc quad_perm:[2,3,0,1] row_mask:0xf bank_mask:0xf\n"
    "v_mfma_f32_16x16x32_bf16 v[68:71], v[164:167], v[136:139], v[68:71]\n"
    "s_waitcnt vmcnt(10)\n"
    "s_mov_b64 vcc, s[4:5]\n"
    "ds_read_b64_tr_b16 v[164:165], v244 offset:8704\n"
    "v_cndmask_b32_dpp v224, v212, v208, vcc quad_perm:[1,0,3,2] row_mask:0xf bank_mask:0xf\n"
    "v_mfma_f32_16x16x32_bf16 v[72:75], v[168:171], v[104:107], v[72:75]\n"
    "ds_read_b64_tr_b16 v[166:167], v244 offset:12800\n"
    "v_cndmask_b32_dpp v232, v220, v216, vcc quad_perm:[1,0,3,2] row_mask:0xf bank_mask:0xf\n"
    "v_cndmask_b32_dpp v225, v213, v209, vcc quad_perm:[1,0,3,2] row_mask:0xf bank_mask:0xf\n"
    "v_cndmask_b32_dpp v233, v221, v217, vcc quad_perm:[1,0,3,2] row_mask:0xf bank_mask:0xf\n"
    "v_mfma_f32_16x16x32_bf16 v[76:79], v[168:171], v[120:123], v[76:79]\n"
    "buffer_load_dwordx4 v246, s[36:39], s70 offen offset:2048 nt lds\n"
    "v_cndmask_b32_dpp v226, v214, v210, vcc quad_perm:[1,0,3,2] row_mask:0xf bank_mask:0xf\n"
    "v_cndmask_b32_dpp v234, v222, v218, vcc quad_perm:[1,0,3,2] row_mask:0xf bank_mask:0xf\n"
    "v_cndmask_b32_dpp v227, v215, v211, vcc quad_perm:[1,0,3,2] row_mask:0xf bank_mask:0xf\n"
    "v_mfma_f32_16x16x32_bf16 v[80:83], v[168:171], v[136:139], v[80:83]\n"
    "ds_read_b64_tr_b16 v[168:169], v243 offset:8960\n"
    "v_cndmask_b32_dpp v235, v223, v219, vcc quad_perm:[1,0,3,2] row_mask:0xf bank_mask:0xf\n"
    "v_mfma_f32_16x16x32_bf16 v[84:87], v[172:175], v[104:107], v[84:87]\n"
    "s_mov_b64 vcc, s[6:7]\n"
    "ds_read_b64_tr_b16 v[170:171], v243 offset:13056\n"
    "v_cndmask_b32_dpp v228, v208, v212, vcc quad_perm:[1,0,3,2] row_mask:0xf bank_mask:0xf\n"
    "v_cndmask_b32_dpp v236, v216, v220, vcc quad_perm:[1,0,3,2] row_mask:0xf bank_mask:0xf\n"
    "v_mfma_f32_16x16x32_bf16 v[88:91], v[172:175], v[120:123], v[88:91]\n"
    "buffer_load_dwordx4 v247, s[36:39], s70 offen offset:3072 nt lds\n"
    "v_cndmask_b32_dpp v229, v209, v213, vcc quad_perm:[1,0,3,2] row_mask:0xf bank_mask:0xf\n"
    "v_cndmask_b32_dpp v237, v217, v221, vcc quad_perm:[1,0,3,2] row_mask:0xf bank_mask:0xf\n"
    "v_cndmask_b32_dpp v230, v210, v214, vcc quad_perm:[1,0,3,2] row_mask:0xf bank_mask:0xf\n"
    "v_mfma_f32_16x16x32_bf16 v[92:95], v[172:175], v[136:139], v[92:95]\n"
    "s_waitcnt lgkmcnt(6)\n"
    "ds_read_b64_tr_b16 v[172:173], v244 offset:8960\n"
    "v_cndmask_b32_dpp v238, v218, v222, vcc quad_perm:[1,0,3,2] row_mask:0xf bank_mask:0xf\n"
    "v_cndmask_b32_dpp v231, v211, v215, vcc quad_perm:[1,0,3,2] row_mask:0xf bank_mask:0xf\n"
    "v_mfma_f32_16x16x32_bf16 v[0:3], v[144:147], v[108:111], v[0:3]\n"
    "s_add_u32 s70, s70, 0x2000\n"
    "ds_read_b64_tr_b16 v[174:175], v244 offset:13056\n"
    "v_cndmask_b32_dpp v239, v219, v223, vcc quad_perm:[1,0,3,2] row_mask:0xf bank_mask:0xf\n"
    "v_mfma_f32_16x16x32_bf16 v[4:7], v[144:147], v[124:127], v[4:7]\n"
    "s_mov_b64 vcc, s[8:9]\n"
    "v_cndmask_b32_dpp v208, v232, v224, vcc quad_perm:[2,3,0,1] row_mask:0xf bank_mask:0xf\n"
    "v_cndmask_b32_dpp v212, v236, v228, vcc quad_perm:[2,3,0,1] row_mask:0xf bank_mask:0xf\n"
    "v_cndmask_b32_dpp v209, v233, v225, vcc quad_perm:[2,3,0,1] row_mask:0xf bank_mask:0xf\n"
    "v_cndmask_b32_dpp v213, v237, v229, vcc quad_perm:[2,3,0,1] row_mask:0xf bank_mask:0xf\n"
    "v_mfma_f32_16x16x32_bf16 v[8:11], v[144:147], v[140:143], v[8:11]\n"
    "ds_read_b64_tr_b16 v[144:145], v243 offset:0\n"
    "v_cndmask_b32_dpp v210, v234, v226, vcc quad_perm:[2,3,0,1] row_mask:0xf bank_mask:0xf\n"
    "v_cndmask_b32_dpp v214, v238, v230, vcc quad_perm:[2,3,0,1] row_mask:0xf bank_mask:0xf\n"
    "v_cndmask_b32_dpp v211, v235, v227, vcc quad_perm:[2,3,0,1] row_mask:0xf bank_mask:0xf\n"
    "v_mfma_f32_16x16x32_bf16 v[12:15], v[148:151], v[108:111], v[12:15]\n"
    "ds_read_b64_tr_b16 v[146:147], v243 offset:4096\n"
    "v_cndmask_b32_dpp v215, v239, v231, vcc quad_perm:[2,3,0,1] row_mask:0xf bank_mask:0xf\n"
    "v_mfma_f32_16x16x32_bf16 v[16:19], v[148:151], v[124:127], v[16:19]\n"
    "s_mov_b64 vcc, s[10:11]\n"
    "v_cndmask_b32_dpp v216, v224, v232, vcc quad_perm:[2,3,0,1] row_mask:0xf bank_mask:0xf\n"
    "v_cndmask_b32_dpp v220, v228, v236, vcc quad_perm:[2,3,0,1] row_mask:0xf bank_mask:0xf\n"
    "v_cndmask_b32_dpp v217, v225, v233, vcc quad_perm:[2,3,0,1] row_mask:0xf bank_mask:0xf\n"
    "v_cndmask_b32_dpp v221, v229, v237, vcc quad_perm:[2,3,0,1] row_mask:0xf bank_mask:0xf\n"
    "v_mfma_f32_16x16x32_bf16 v[20:23], v[148:151], v[140:143], v[20:23]\n"
    "ds_read_b64_tr_b16 v[148:149], v244 offset:0\n"
    "v_cndmask_b32_dpp v218, v226, v234, vcc quad_perm:[2,3,0,1] row_mask:0xf bank_mask:0xf\n"
    "v_cndmask_b32_dpp v222, v230, v238, vcc quad_perm:[2,3,0,1] row_mask:0xf bank_mask:0xf\n"
    "v_cndmask_b32_dpp v219, v227, v235, vcc quad_perm:[2,3,0,1] row_mask:0xf bank_mask:0xf\n"
    "v_mfma_f32_16x16x32_bf16 v[24:27], v[152:155], v[108:111], v[24:27]\n"
    "ds_read_b64_tr_b16 v[150:151], v244 offset:4096\n"
    "v_cndmask_b32_dpp v223, v231, v239, vcc quad_perm:[2,3,0,1] row_mask:0xf bank_mask:0xf\n"
    "v_mfma_f32_16x16x32_bf16 v[28:31], v[152:155], v[124:127], v[28:31]\n"
    "v_mfma_f32_16x16x32_bf16 v[32:35], v[152:155], v[140:143], v[32:35]\n"
    "ds_read_b64_tr_b16 v[152:153], v243 offset:256\n"
    "v_mfma_f32_16x16x32_bf16 v[36:39], v[156:159], v[108:111], v[36:39]\n"
    "ds_read_b64_tr_b16 v[154:155], v243 offset:4352\n"
    "v_mfma_f32_16x16x32_bf16 v[40:43], v[156:159], v[124:127], v[40:43]\n"
    "v_mfma_f32_16x16x32_bf16 v[44:47], v[156:159], v[140:143], v[44:47]\n"
    "s_waitcnt lgkmcnt(6)\n"
    "ds_read_b64_tr_b16 v[156:157], v244 offset:256\n"
    "v_mfma_f32_16x16x32_bf16 v[48:51], v[160:163], v[108:111], v[48:51]\n"
    "ds_read_b64_tr_b16 v[158:159], v244 offset:4352\n"
    "v_mfma_f32_16x16x32_bf16 v[52:55], v[160:163], v[124:127], v[52:55]\n"
    "v_mfma_f32_16x16x32_bf16 v[56:59], v[160:163], v[140:143], v[56:59]\n"
    "ds_read_b64_tr_b16 v[160:161], v243 offset:512\n"
    "v_mfma_f32_16x16x32_bf16 v[60:63], v[164:167], v[108:111], v[60:63]\n"
    "ds_read_b64_tr_b16 v[162:163], v243 offset:4608\n"
    "v_mfma_f32_16x16x32_bf16 v[64:67], v[164:167], v[124:127], v[64:67]\n"
    "v_mfma_f32_16x16x32_bf16 v[68:71], v[164:167], v[140:143], v[68:71]\n"
    "ds_read_b64_tr_b16 v[164:165], v244 offset:512\n"
    "v_mfma_f32_16x16x32_bf16 v[72:75], v[168:171], v[108:111], v[72:75]\n"
    "ds_read_b64_tr_b16 v[166:167], v244 offset:4608\n"
    "v_mfma_f32_16x16x32_bf16 v[76:79], v[168:171], v[124:127], v[76:79]\n"
    "v_mfma_f32_16x16x32_bf16 v[80:83], v[168:171], v[140:143], v[80:83]\n"
    "ds_read_b64_tr_b16 v[168:169], v243 offset:768\n"
    "v_mfma_f32_16x16x32_bf16 v[84:87], v[172:175], v[108:111], v[84:87]\n"
    "ds_read_b64_tr_b16 v[170:171], v243 offset:4864\n"
    "v_mfma_f32_16x16x32_bf16 v[88:91], v[172:175], v[124:127], v[88:91]\n"
    "v_mfma_f32_16x16x32_bf16 v[92:95], v[172:175], v[140:143], v[92:95]\n"
    "s_waitcnt lgkmcnt(10)\n"
    "ds_read_b64_tr_b16 v[172:173], v244 offset:768\n"
    "v_mfma_f32_16x16x32_bf16 v[0:3], v[144:147], v[176:179], v[0:3]\n"
    "ds_read_b64_tr_b16 v[174:175], v244 offset:4864\n"
    "v_mfma_f32_16x16x32_bf16 v[4:7], v[144:147], v[192:195], v[4:7]\n"
    "v_add_u32 v243, 16384, v243\n"
    "v_add_u32 v244, 16384, v244\n"
    "v_mfma_f32_16x16x32_bf16 v[8:11], v[144:147], v[208:211], v[8:11]\n"
    "v_mfma_f32_16x16x32_bf16 v[12:15], v[148:151], v[176:179], v[12:15]\n"
    "v_mfma_f32_16x16x32_bf16 v[16:19], v[148:151], v[192:195], v[16:19]\n"
    "s_waitcnt lgkmcnt(0)\n"
    "s_barrier\n"
    "v_mfma_f32_16x16x32_bf16 v[20:23], v[148:151], v[208:211], v[20:23]\n"
    "ds_read_b64_tr_b16 v[144:145], v243 offset:8192\n"
    "v_mfma_f32_16x16x32_bf16 v[24:27], v[152:155], v[176:179], v[24:27]\n"
    "s_add_u32 m0, s68, 16384\n"
    "ds_read_b64_tr_b16 v[146:147], v243 offset:12288\n"
    "v_mfma_f32_16x16x32_bf16 v[28:31], v[152:155], v[192:195], v[28:31]\n"
    "ds_read_b64_tr_b16 v[148:149], v244 offset:8192\n"
    "v_mfma_f32_16x16x32_bf16 v[32:35], v[152:155], v[208:211], v[32:35]\n"
    "ds_read_b64_tr_b16 v[150:151], v244 offset:12288\n"
    "v_mfma_f32_16x16x32_bf16 v[36:39], v[156:159], v[176:179], v[36:39]\n"
    "ds_read_b64_tr_b16 v[152:153], v243 offset:8448\n"
    "v_mfma_f32_16x16x32_bf16 v[40:43], v[156:159], v[192:195], v[40:43]\n"
    "ds_read_b64_tr_b16 v[154:155], v243 offset:12544\n"
    "v_mfma_f32_16x16x32_bf16 v[44:47], v[156:159], v[208:211], v[44:47]\n"
    "ds_read_b64_tr_b16 v[156:157], v244 offset:8448\n"
    "v_mfma_f32_16x16x32_bf16 v[48:51], v[160:163], v[176:179], v[48:51]\n"
    "ds_read_b64_tr_b16 v[158:159], v244 offset:12544\n"
    "v_mfma_f32_16x16x32_bf16 v[52:55], v[160:163], v[192:195], v[52:55]\n"
    "buffer_load_dwordx4 v246, s[36:39], s70 offen offset:0 nt lds\n"
    "v_mfma_f32_16x16x32_bf16 v[56:59], v[160:163], v[208:211], v[56:59]\n"
    "ds_read_b64_tr_b16 v[160:161], v243 offset:8704\n"
    "v_mfma_f32_16x16x32_bf16 v[60:63], v[164:167], v[176:179], v[60:63]\n"
    "ds_read_b64_tr_b16 v[162:163], v243 offset:12800\n"
    "v_mfma_f32_16x16x32_bf16 v[64:67], v[164:167], v[192:195], v[64:67]\n"
    "buffer_load_dwordx4 v247, s[36:39], s70 offen offset:1024 nt lds\n"
    "v_mfma_f32_16x16x32_bf16 v[68:71], v[164:167], v[208:211], v[68:71]\n"
    "ds_read_b64_tr_b16 v[164:165], v244 offset:8704\n"
    "v_mfma_f32_16x16x32_bf16 v[72:75], v[168:171], v[176:179], v[72:75]\n"
    "ds_read_b64_tr_b16 v[166:167], v244 offset:12800\n"
    "v_mfma_f32_16x16x32_bf16 v[76:79], v[168:171], v[192:195], v[76:79]\n"
    "buffer_load_dwordx4 v246, s[36:39], s70 offen offset:2048 nt lds\n"
    "v_mfma_f32_16x16x32_bf16 v[80:83], v[168:171], v[208:211], v[80:83]\n"
    "ds_read_b64_tr_b16 v[168:169], v243 offset:8960\n"
    "v_mfma_f32_16x16x32_bf16 v[84:87], v[172:175], v[176:179], v[84:87]\n"
    "ds_read_b64_tr_b16 v[170:171], v243 offset:13056\n"
    "v_mfma_f32_16x16x32_bf16 v[88:91], v[172:175], v[192:195], v[88:91]\n"
    "buffer_load_dwordx4 v247, s[36:39], s70 offen offset:3072 nt lds\n"
    "v_mfma_f32_16x16x32_bf16 v[92:95], v[172:175], v[208:211], v[92:95]\n"
    "s_waitcnt lgkmcnt(6)\n"
    "ds_read_b64_tr_b16 v[172:173], v244 offset:8960\n"
    "v_mfma_f32_16x16x32_bf16 v[0:3], v[144:147], v[180:183], v[0:3]\n"
    "s_add_u32 s70, s70, 0x2000\n"
    "ds_read_b64_tr_b16 v[174:175], v244 offset:13056\n"
    "v_mfma_f32_16x16x32_bf16 v[4:7], v[144:147], v[196:199], v[4:7]\n"
    "v_mfma_f32_16x16x32_bf16 v[8:11], v[144:147], v[212:215], v[8:11]\n"
    "ds_read_b64_tr_b16 v[144:145], v243 offset:0\n"
    "v_mfma_f32_16x16x32_bf16 v[12:15], v[148:151], v[180:183], v[12:15]\n"
    "ds_read_b64_tr_b16 v[146:147], v243 offset:4096\n"
    "v_mfma_f32_16x16x32_bf16 v[16:19], v[148:151], v[196:199], v[16:19]\n"
    "v_mfma_f32_16x16x32_bf16 v[20:23], v[148:151], v[212:215], v[20:23]\n"
    "ds_read_b64_tr_b16 v[148:149], v244 offset:0\n"
    "v_mfma_f32_16x16x32_bf16 v[24:27], v[152:155], v[180:183], v[24:27]\n"
    "ds_read_b64_tr_b16 v[150:151], v244 offset:4096\n"
    "v_mfma_f32_16x16x32_bf16 v[28:31], v[152:155], v[196:199], v[28:31]\n"
    "v_mfma_f32_16x16x32_bf16 v[32:35], v[152:155], v[212:215], v[32:35]\n"
    "ds_read_b64_tr_b16 v[152:153], v243 offset:256\n"
    "v_mfma_f32_16x16x32_bf16 v[36:39], v[156:159], v[180:183], v[36:39]\n"
    "ds_read_b64_tr_b16 v[154:155], v243 offset:4352\n"
    "v_mfma_f32_16x16x32_bf16 v[40:43], v[156:159], v[196:199], v[40:43]\n"
    "v_mfma_f32_16x16x32_bf16 v[44:47], v[156:159], v[212:215], v[44:47]\n"
    "s_waitcnt lgkmcnt(6)\n"
    "ds_read_b64_tr_b16 v[156:157], v244 offset:256\n"
    "v_mfma_f32_16x16x32_bf16 v[48:51], v[160:163], v[180:183], v[48:51]\n"
    "ds_read_b64_tr_b16 v[158:159], v244 offset:4352\n"
    "v_mfma_f32_16x16x32_bf16 v[52:55], v[160:163], v[196:199], v[52:55]\n"
    "v_mfma_f32_16x16x32_bf16 v[56:59], v[160:163], v[212:215], v[56:59]\n"
    "ds_read_b64_tr_b16 v[160:161], v243 offset:512\n"
    "v_mfma_f32_16x16x32_bf16 v[60:63], v[164:167], v[180:183], v[60:63]\n"
    "ds_read_b64_tr_b16 v[162:163], v243 offset:4608\n"
    "v_mfma_f32_16x16x32_bf16 v[64:67], v[164:167], v[196:199], v[64:67]\n"
    "v_mfma_f32_16x16x32_bf16 v[68:71], v[164:167], v[212:215], v[68:71]\n"
    "ds_read_b64_tr_b16 v[164:165], v244 offset:512\n"
    "v_mfma_f32_16x16x32_bf16 v[72:75], v[168:171], v[180:183], v[72:75]\n"
    "ds_read_b64_tr_b16 v[166:167], v244 offset:4608\n"
    "v_mfma_f32_16x16x32_bf16 v[76:79], v[168:171], v[196:199], v[76:79]\n"
    "v_mfma_f32_16x16x32_bf16 v[80:83], v[168:171], v[212:215], v[80:83]\n"
    "ds_read_b64_tr_b16 v[168:169], v243 offset:768\n"
    "v_mfma_f32_16x16x32_bf16 v[84:87], v[172:175], v[180:183], v[84:87]\n"
    "ds_read_b64_tr_b16 v[170:171], v243 offset:4864\n"
    "v_mfma_f32_16x16x32_bf16 v[88:91], v[172:175], v[196:199], v[88:91]\n"
    "v_mfma_f32_16x16x32_bf16 v[92:95], v[172:175], v[212:215], v[92:95]\n"
    "s_waitcnt lgkmcnt(10)\n"
    "ds_read_b64_tr_b16 v[172:173], v244 offset:768\n"
    "v_mfma_f32_16x16x32_bf16 v[0:3], v[144:147], v[184:187], v[0:3]\n"
    "ds_read_b64_tr_b16 v[174:175], v244 offset:4864\n"
    "v_mfma_f32_16x16x32_bf16 v[4:7], v[144:147], v[200:203], v[4:7]\n"
    "v_add_u32 v243, 16384, v243\n"
    "v_add_u32 v244, 16384, v244\n"
    "v_mfma_f32_16x16x32_bf16 v[8:11], v[144:147], v[216:219], v[8:11]\n"
    "v_mfma_f32_16x16x32_bf16 v[12:15], v[148:151], v[184:187], v[12:15]\n"
    "v_mfma_f32_16x16x32_bf16 v[16:19], v[148:151], v[200:203], v[16:19]\n"
    "s_waitcnt vmcnt(12)\n"
    "s_waitcnt lgkmcnt(0)\n"
    "s_barrier\n"
    "v_mfma_f32_16x16x32_bf16 v[20:23], v[148:151], v[216:219], v[20:23]\n"
    "ds_read_b64_tr_b16 v[144:145], v243 offset:8192\n"
    "v_mfma_f32_16x16x32_bf16 v[24:27], v[152:155], v[184:187], v[24:27]\n"
    "s_add_u32 m0, s68, 32768\n"
    "ds_read_b64_tr_b16 v[146:147], v243 offset:12288\n"
    "v_mfma_f32_16x16x32_bf16 v[28:31], v[152:155], v[200:203], v[28:31]\n"
    "ds_read_b64_tr_b16 v[148:149], v244 offset:8192\n"
    "v_mfma_f32_16x16x32_bf16 v[32:35], v[152:155], v[216:219], v[32:35]\n"
    "ds_read_b64_tr_b16 v[150:151], v244 offset:12288\n"
    "v_mfma_f32_16x16x32_bf16 v[36:39], v[156:159], v[184:187], v[36:39]\n"
    "ds_read_b64_tr_b16 v[152:153], v243 offset:8448\n"
    "v_mfma_f32_16x16x32_bf16 v[40:43], v[156:159], v[200:203], v[40:43]\n"
    "ds_read_b64_tr_b16 v[154:155], v243 offset:12544\n"
    "v_mfma_f32_16x16x32_bf16 v[44:47], v[156:159], v[216:219], v[44:47]\n"
    "ds_read_b64_tr_b16 v[156:157], v244 offset:8448\n"
    "v_mfma_f32_16x16x32_bf16 v[48:51], v[160:163], v[184:187], v[48:51]\n"
    "ds_read_b64_tr_b16 v[158:159], v244 offset:12544\n"
    "v_mfma_f32_16x16x32_bf16 v[52:55], v[160:163], v[200:203], v[52:55]\n"
    "buffer_load_dwordx4 v246, s[36:39], s70 offen offset:0 nt lds\n"
    "v_mfma_f32_16x16x32_bf16 v[56:59], v[160:163], v[216:219], v[56:59]\n"
    "ds_read_b64_tr_b16 v[160:161], v243 offset:8704\n"
    "v_mfma_f32_16x16x32_bf16 v[60:63], v[164:167], v[184:187], v[60:63]\n"
    "ds_read_b64_tr_b16 v[162:163], v243 offset:12800\n"
    "v_mfma_f32_16x16x32_bf16 v[64:67], v[164:167], v[200:203], v[64:67]\n"
    "buffer_load_dwordx4 v247, s[36:39], s70 offen offset:1024 nt lds\n"
    "v_mfma_f32_16x16x32_bf16 v[68:71], v[164:167], v[216:219], v[68:71]\n"
    "ds_read_b64_tr_b16 v[164:165], v244 offset:8704\n"
    "v_mfma_f32_16x16x32_bf16 v[72:75], v[168:171], v[184:187], v[72:75]\n"
    "ds_read_b64_tr_b16 v[166:167], v244 offset:12800\n"
    "v_mfma_f32_16x16x32_bf16 v[76:79], v[168:171], v[200:203], v[76:79]\n"
    "buffer_load_dwordx4 v246, s[36:39], s70 offen offset:2048 nt lds\n"
    "v_mfma_f32_16x16x32_bf16 v[80:83], v[168:171], v[216:219], v[80:83]\n"
    "ds_read_b64_tr_b16 v[168:169], v243 offset:8960\n"
    "v_mfma_f32_16x16x32_bf16 v[84:87], v[172:175], v[184:187], v[84:87]\n"
    "ds_read_b64_tr_b16 v[170:171], v243 offset:13056\n"
    "v_mfma_f32_16x16x32_bf16 v[88:91], v[172:175], v[200:203], v[88:91]\n"
    "buffer_load_dwordx4 v247, s[36:39], s70 offen offset:3072 nt lds\n"
    "v_mfma_f32_16x16x32_bf16 v[92:95], v[172:175], v[216:219], v[92:95]\n"
    "s_waitcnt lgkmcnt(6)\n"
    "ds_read_b64_tr_b16 v[172:173], v244 offset:8960\n"
    "v_mfma_f32_16x16x32_bf16 v[0:3], v[144:147], v[188:191], v[0:3]\n"
    "s_add_u32 s70, s70, 0x2000\n"
    "ds_read_b64_tr_b16 v[174:175], v244 offset:13056\n"
    "v_mfma_f32_16x16x32_bf16 v[4:7], v[144:147], v[204:207], v[4:7]\n"
    "v_mfma_f32_16x16x32_bf16 v[8:11], v[144:147], v[220:223], v[8:11]\n"
    "ds_read_b128 v[144:147], v245 offset:0\n"
    "v_mfma_f32_16x16x32_bf16 v[12:15], v[148:151], v[188:191], v[12:15]\n"
    "v_mfma_f32_16x16x32_bf16 v[16:19], v[148:151], v[204:207], v[16:19]\n"
    "v_mfma_f32_16x16x32_bf16 v[20:23], v[148:151], v[220:223], v[20:23]\n"
    "ds_read_b128 v[148:151], v245 offset:256\n"
    "v_mfma_f32_16x16x32_bf16 v[24:27], v[152:155], v[188:191], v[24:27]\n"
    "v_mfma_f32_16x16x32_bf16 v[28:31], v[152:155], v[204:207], v[28:31]\n"
    "v_mfma_f32_16x16x32_bf16 v[32:35], v[152:155], v[220:223], v[32:35]\n"
    "ds_read_b128 v[152:155], v245 offset:512\n"
    "v_mfma_f32_16x16x32_bf16 v[36:39], v[156:159], v[188:191], v[36:39]\n"
    "v_mul_f32 v0, s86, v0\n"
    "v_mul_f32 v1, s86, v1\n"
    "v_mfma_f32_16x16x32_bf16 v[40:43], v[156:159], v[204:207], v[40:43]\n"
    "v_mul_f32 v2, s86, v2\n"
    "v_mul_f32 v3, s86, v3\n"
    "v_mfma_f32_16x16x32_bf16 v[44:47], v[156:159], v[220:223], v[44:47]\n"
    "s_waitcnt lgkmcnt(3)\n"
    "ds_read_b128 v[156:159], v245 offset:768\n"
    "v_mfma_f32_16x16x32_bf16 v[48:51], v[160:163], v[188:191], v[48:51]\n"
    "v_mul_f32 v12, s86, v12\n"
    "v_mul_f32 v13, s86, v13\n"
    "v_mfma_f32_16x16x32_bf16 v[52:55], v[160:163], v[204:207], v[52:55]\n"
    "v_mul_f32 v14, s86, v14\n"
    "v_mul_f32 v15, s86, v15\n"
    "v_mfma_f32_16x16x32_bf16 v[56:59], v[160:163], v[220:223], v[56:59]\n"
    "ds_read_b128 v[160:163], v245 offset:4096\n"
    "v_cvt_pk_bf16_f32 v224, v0, v1\n"
    "v_mfma_f32_16x16x32_bf16 v[60:63], v[164:167], v[188:191], v[60:63]\n"
    "v_cvt_pk_bf16_f32 v225, v2, v3\n"
    "v_cvt_pk_bf16_f32 v226, v12, v13\n"
    "v_mfma_f32_16x16x32_bf16 v[64:67], v[164:167], v[204:207], v[64:67]\n"
    "v_cvt_pk_bf16_f32 v227, v14, v15\n"
    "v_cndmask_b32_e64 v96, v224, v226, s[84:85]\n"
    "v_mfma_f32_16x16x32_bf16 v[68:71], v[164:167], v[220:223], v[68:71]\n"
    "ds_read_b128 v[164:167], v245 offset:4352\n"
    "v_cndmask_b32_e64 v97, v225, v227, s[84:85]\n"
    "v_mfma_f32_16x16x32_bf16 v[72:75], v[168:171], v[188:191], v[72:75]\n"
    "v_cndmask_b32_e64 v98, v226, v224, s[84:85]\n"
    "v_cndmask_b32_e64 v99, v227, v225, s[84:85]\n"
    "v_mfma_f32_16x16x32_bf16 v[76:79], v[168:171], v[204:207], v[76:79]\n"
    "v_mov_b64 v[0:1], 0\n"
    "v_mov_b64 v[2:3], 0\n"
    "v_mfma_f32_16x16x32_bf16 v[80:83], v[168:171], v[220:223], v[80:83]\n"
    "ds_read_b128 v[168:171], v245 offset:4608\n"
    "v_mov_b64 v[12:13], 0\n"
    "v_mfma_f32_16x16x32_bf16 v[84:87], v[172:175], v[188:191], v[84:87]\n"
    "v_mov_b64 v[14:15], 0\n"
    "v_mul_f32 v4, s86, v4\n"
    "v_mfma_f32_16x16x32_bf16 v[88:91], v[172:175], v[204:207], v[88:91]\n"
    "v_mul_f32 v5, s86, v5\n"
    "v_mul_f32 v6, s86, v6\n"
    "v_mfma_f32_16x16x32_bf16 v[92:95], v[172:175], v[220:223], v[92:95]\n"
    "ds_read_b128 v[172:175], v245 offset:4864\n"
    "v_mul_f32 v7, s86, v7\n"
    "v_mul_f32 v16, s86, v16\n"
    "v_mul_f32 v17, s86, v17\n"
    "v_mul_f32 v18, s86, v18\n"
    "v_mul_f32 v19, s86, v19\n"
    "v_cvt_pk_bf16_f32 v228, v4, v5\n"
    "v_cvt_pk_bf16_f32 v229, v6, v7\n"
    "v_cvt_pk_bf16_f32 v230, v16, v17\n"
    "v_cvt_pk_bf16_f32 v231, v18, v19\n"
    "v_cndmask_b32_e64 v100, v228, v230, s[84:85]\n"
    "v_cndmask_b32_e64 v101, v229, v231, s[84:85]\n"
    "v_cndmask_b32_e64 v102, v230, v228, s[84:85]\n"
    "v_cndmask_b32_e64 v103, v231, v229, s[84:85]\n"
    "v_mov_b64 v[4:5], 0\n"
    "v_mov_b64 v[6:7], 0\n"
    "v_mov_b64 v[16:17], 0\n"
    "v_mov_b64 v[18:19], 0\n"
    "v_mul_f32 v8, s86, v8\n"
    "v_mul_f32 v9, s86, v9\n"
    "v_mul_f32 v10, s86, v10\n"
    "v_mul_f32 v11, s86, v11\n"
    "v_mul_f32 v20, s86, v20\n"
    "v_mul_f32 v21, s86, v21\n"
    "v_mul_f32 v22, s86, v22\n"
    "v_mul_f32 v23, s86, v23\n"
    "v_cvt_pk_bf16_f32 v232, v8, v9\n"
    "v_cvt_pk_bf16_f32 v233, v10, v11\n"
    "v_cvt_pk_bf16_f32 v234, v20, v21\n"
    "v_cvt_pk_bf16_f32 v235, v22, v23\n"
    "v_cndmask_b32_e64 v104, v232, v234, s[84:85]\n"
    "v_cndmask_b32_e64 v105, v233, v235, s[84:85]\n"
    "v_cndmask_b32_e64 v106, v234, v232, s[84:85]\n"
    "v_cndmask_b32_e64 v107, v235, v233, s[84:85]\n"
    "v_mov_b64 v[8:9], 0\n"
    "v_mov_b64 v[10:11], 0\n"
    "v_mov_b64 v[20:21], 0\n"
    "v_mov_b64 v[22:23], 0\n"
    "v_mul_f32 v24, s86, v24\n"
    "v_mul_f32 v25, s86, v25\n"
    "v_mul_f32 v26, s86, v26\n"
    "v_mul_f32 v27, s86, v27\n"
    "v_mul_f32 v36, s86, v36\n"
    "v_mul_f32 v37, s86, v37\n"
    "v_mul_f32 v38, s86, v38\n"
    "v_mul_f32 v39, s86, v39\n"
    "v_cvt_pk_bf16_f32 v236, v24, v25\n"
    "v_cvt_pk_bf16_f32 v237, v26, v27\n"
    "v_cvt_pk_bf16_f32 v238, v36, v37\n"
    "v_cvt_pk_bf16_f32 v239, v38, v39\n"
    "v_cndmask_b32_e64 v108, v236, v238, s[84:85]\n"
    "v_cndmask_b32_e64 v109, v237, v239, s[84:85]\n"
    "v_cndmask_b32_e64 v110, v238, v236, s[84:85]\n"
    "v_cndmask_b32_e64 v111, v239, v237, s[84:85]\n"
    "v_mov_b64 v[24:25], 0\n"
    "v_mov_b64 v[26:27], 0\n"
    "v_mov_b64 v[36:37], 0\n"
    "v_mov_b64 v[38:39], 0\n"
    "v_mul_f32 v28, s86, v28\n"
    "v_mul_f32 v29, s86, v29\n"
    "v_mul_f32 v30, s86, v30\n"
    "v_mul_f32 v31, s86, v31\n"
    "v_mul_f32 v40, s86, v40\n"
    "v_mul_f32 v41, s86, v41\n"
    "v_mul_f32 v42, s86, v42\n"
    "v_mul_f32 v43, s86, v43\n"
    "v_cvt_pk_bf16_f32 v224, v28, v29\n"
    "v_cvt_pk_bf16_f32 v225, v30, v31\n"
    "v_cvt_pk_bf16_f32 v226, v40, v41\n"
    "v_cvt_pk_bf16_f32 v227, v42, v43\n"
    "v_cndmask_b32_e64 v112, v224, v226, s[84:85]\n"
    "v_cndmask_b32_e64 v113, v225, v227, s[84:85]\n"
    "v_cndmask_b32_e64 v114, v226, v224, s[84:85]\n"
    "v_cndmask_b32_e64 v115, v227, v225, s[84:85]\n"
    "v_mov_b64 v[28:29], 0\n"
    "v_mov_b64 v[30:31], 0\n"
    "v_mov_b64 v[40:41], 0\n"
    "v_mov_b64 v[42:43], 0\n"
    "v_mul_f32 v32, s86, v32\n"
    "v_mul_f32 v33, s86, v33\n"
    "v_mul_f32 v34, s86, v34\n"
    "v_mul_f32 v35, s86, v35\n"
    "v_mul_f32 v44, s86, v44\n"
    "v_mul_f32 v45, s86, v45\n"
    "v_mul_f32 v46, s86, v46\n"
    "v_mul_f32 v47, s86, v47\n"
    "v_cvt_pk_bf16_f32 v228, v32, v33\n"
    "v_cvt_pk_bf16_f32 v229, v34, v35\n"
    "v_cvt_pk_bf16_f32 v230, v44, v45\n"
    "v_cvt_pk_bf16_f32 v231, v46, v47\n"
    "v_cndmask_b32_e64 v116, v228, v230, s[84:85]\n"
    "v_cndmask_b32_e64 v117, v229, v231, s[84:85]\n"
    "v_cndmask_b32_e64 v118, v230, v228, s[84:85]\n"
    "v_cndmask_b32_e64 v119, v231, v229, s[84:85]\n"
    "v_mov_b64 v[32:33], 0\n"
    "v_mov_b64 v[34:35], 0\n"
    "v_mov_b64 v[44:45], 0\n"
    "v_mov_b64 v[46:47], 0\n"
    "v_mul_f32 v48, s86, v48\n"
    "v_mul_f32 v49, s86, v49\n"
    "v_mul_f32 v50, s86, v50\n"
    "v_mul_f32 v51, s86, v51\n"
    "v_mul_f32 v60, s86, v60\n"
    "v_mul_f32 v61, s86, v61\n"
    "v_mul_f32 v62, s86, v62\n"
    "v_mul_f32 v63, s86, v63\n"
    "v_cvt_pk_bf16_f32 v232, v48, v49\n"
    "v_cvt_pk_bf16_f32 v233, v50, v51\n"
    "v_cvt_pk_bf16_f32 v234, v60, v61\n"
    "v_cvt_pk_bf16_f32 v235, v62, v63\n"
    "v_cndmask_b32_e64 v120, v232, v234, s[84:85]\n"
    "v_cndmask_b32_e64 v121, v233, v235, s[84:85]\n"
    "v_cndmask_b32_e64 v122, v234, v232, s[84:85]\n"
    "v_cndmask_b32_e64 v123, v235, v233, s[84:85]\n"
    "v_mov_b64 v[48:49], 0\n"
    "v_mov_b64 v[50:51], 0\n"
    "v_mov_b64 v[60:61], 0\n"
    "v_mov_b64 v[62:63], 0\n"
    "v_mul_f32 v52, s86, v52\n"
    "v_mul_f32 v53, s86, v53\n"
    "v_mul_f32 v54, s86, v54\n"
    "v_mul_f32 v55, s86, v55\n"
    "v_mul_f32 v64, s86, v64\n"
    "v_mul_f32 v65, s86, v65\n"
    "v_mul_f32 v66, s86, v66\n"
    "v_mul_f32 v67, s86, v67\n"
    "v_cvt_pk_bf16_f32 v236, v52, v53\n"
    "v_cvt_pk_bf16_f32 v237, v54, v55\n"
    "v_cvt_pk_bf16_f32 v238, v64, v65\n"
    "v_cvt_pk_bf16_f32 v239, v66, v67\n"
    "v_cndmask_b32_e64 v124, v236, v238, s[84:85]\n"
    "v_cndmask_b32_e64 v125, v237, v239, s[84:85]\n"
    "v_cndmask_b32_e64 v126, v238, v236, s[84:85]\n"
    "v_cndmask_b32_e64 v127, v239, v237, s[84:85]\n"
    "v_mov_b64 v[52:53], 0\n"
    "v_mov_b64 v[54:55], 0\n"
    "v_mov_b64 v[64:65], 0\n"
    "v_mov_b64 v[66:67], 0\n"
    "v_mul_f32 v56, s86, v56\n"
    "v_mul_f32 v57, s86, v57\n"
    "v_mul_f32 v58, s86, v58\n"
    "v_mul_f32 v59, s86, v59\n"
    "v_mul_f32 v68, s86, v68\n"
    "v_mul_f32 v69, s86, v69\n"
    "v_mul_f32 v70, s86, v70\n"
    "v_mul_f32 v71, s86, v71\n"
    "v_cvt_pk_bf16_f32 v224, v56, v57\n"
    "v_cvt_pk_bf16_f32 v225, v58, v59\n"
    "v_cvt_pk_bf16_f32 v226, v68, v69\n"
    "v_cvt_pk_bf16_f32 v227, v70, v71\n"
    "v_cndmask_b32_e64 v128, v224, v226, s[84:85]\n"
    "v_cndmask_b32_e64 v129, v225, v227, s[84:85]\n"
    "v_cndmask_b32_e64 v130, v226, v224, s[84:85]\n"
    "v_cndmask_b32_e64 v131, v227, v225, s[84:85]\n"
    "v_mov_b64 v[56:57], 0\n"
    "v_mov_b64 v[58:59], 0\n"
    "v_mov_b64 v[68:69], 0\n"
    "v_mov_b64 v[70:71], 0\n"
    "v_mul_f32 v72, s86, v72\n"
    "v_mul_f32 v73, s86, v73\n"
    "v_mul_f32 v74, s86, v74\n"
    "v_mul_f32 v75, s86, v75\n"
    "v_mul_f32 v84, s86, v84\n"
    "v_mul_f32 v85, s86, v85\n"
    "v_mul_f32 v86, s86, v86\n"
    "v_mul_f32 v87, s86, v87\n"
    "v_cvt_pk_bf16_f32 v228, v72, v73\n"
    "v_cvt_pk_bf16_f32 v229, v74, v75\n"
    "v_cvt_pk_bf16_f32 v230, v84, v85\n"
    "v_cvt_pk_bf16_f32 v231, v86, v87\n"
    "v_cndmask_b32_e64 v132, v228, v230, s[84:85]\n"
    "v_cndmask_b32_e64 v133, v229, v231, s[84:85]\n"
    "v_cndmask_b32_e64 v134, v230, v228, s[84:85]\n"
    "v_cndmask_b32_e64 v135, v231, v229, s[84:85]\n"
    "v_mov_b64 v[72:73], 0\n"
    "v_mov_b64 v[74:75], 0\n"
    "v_mov_b64 v[84:85], 0\n"
    "v_mov_b64 v[86:87], 0\n"
    "v_mul_f32 v76, s86, v76\n"
    "v_mul_f32 v77, s86, v77\n"
    "v_mul_f32 v78, s86, v78\n"
    "v_mul_f32 v79, s86, v79\n"
    "v_mul_f32 v88, s86, v88\n"
    "v_mul_f32 v89, s86, v89\n"
    "v_mul_f32 v90, s86, v90\n"
    "v_mul_f32 v91, s86, v91\n"
    "v_cvt_pk_bf16_f32 v232, v76, v77\n"
    "v_cvt_pk_bf16_f32 v233, v78, v79\n"
    "v_cvt_pk_bf16_f32 v234, v88, v89\n"
    "v_cvt_pk_bf16_f32 v235, v90, v91\n"
    "v_cndmask_b32_e64 v136, v232, v234, s[84:85]\n"
    "v_cndmask_b32_e64 v137, v233, v235, s[84:85]\n"
    "v_cndmask_b32_e64 v138, v234, v232, s[84:85]\n"
    "v_cndmask_b32_e64 v139, v235, v233, s[84:85]\n"
    "v_mov_b64 v[76:77], 0\n"
    "v_mov_b64 v[78:79], 0\n"
    "v_mov_b64 v[88:89], 0\n"
    "v_mov_b64 v[90:91], 0\n"
    "v_mul_f32 v80, s86, v80\n"
    "v_mul_f32 v81, s86, v81\n"
    "v_mul_f32 v82, s86, v82\n"
    "v_mul_f32 v83, s86, v83\n"
    "v_mul_f32 v92, s86, v92\n"
    "v_mul_f32 v93, s86, v93\n"
    "v_mul_f32 v94, s86, v94\n"
    "v_mul_f32 v95, s86, v95\n"
    "v_cvt_pk_bf16_f32 v236, v80, v81\n"
    "v_cvt_pk_bf16_f32 v237, v82, v83\n"
    "v_cvt_pk_bf16_f32 v238, v92, v93\n"
    "v_cvt_pk_bf16_f32 v239, v94, v95\n"
    "v_cndmask_b32_e64 v140, v236, v238, s[84:85]\n"
    "v_cndmask_b32_e64 v141, v237, v239, s[84:85]\n"
    "v_cndmask_b32_e64 v142, v238, v236, s[84:85]\n"
    "v_cndmask_b32_e64 v143, v239, v237, s[84:85]\n"
    "v_mov_b64 v[80:81], 0\n"
    "v_mov_b64 v[82:83], 0\n"
    "v_mov_b64 v[92:93], 0\n"
    "v_mov_b64 v[94:95], 0\n"
    "v_mov_b32 v236, 0x3f803f80\n"
    "v_mov_b32 v237, 0x3f803f80\n"
    "v_mov_b32 v238, 0x3f803f80\n"
    "v_mov_b32 v239, 0x3f803f80\n"
    "v_mov_b32 v240, 0\n"
    "v_mov_b32 v241, 0\n"
    "v_mov_b32 v242, 0\n"
    "v_add_u32 v245, 16384, v245\n"
    "s_waitcnt lgkmcnt(7)\n"
    "v_mfma_f32_16x16x32_bf16 v[176:179], v[144:147], v[96:99], 0\n"
    "s_waitcnt lgkmcnt(6)\n"
    "v_mfma_f32_16x16x32_bf16 v[176:179], v[148:151], v[108:111], v[176:179]\n"
    "s_waitcnt lgkmcnt(5)\n"
    "v_mfma_f32_16x16x32_bf16 v[176:179], v[152:155], v[120:123], v[176:179]\n"
    "s_waitcnt lgkmcnt(4)\n"
    "v_mfma_f32_16x16x32_bf16 v[176:179], v[156:159], v[132:135], v[176:179]\n"
    "v_mfma_f32_16x16x32_bf16 v[180:183], v[144:147], v[100:103], 0\n"
    "v_mfma_f32_16x16x32_bf16 v[180:183], v[148:151], v[112:115], v[180:183]\n"
    "v_mfma_f32_16x16x32_bf16 v[180:183], v[152:155], v[124:127], v[180:183]\n"
    "v_mfma_f32_16x16x32_bf16 v[180:183], v[156:159], v[136:139], v[180:183]\n"
    "v_mfma_f32_16x16x32_bf16 v[184:187], v[144:147], v[104:107], 0\n"
    "v_mfma_f32_16x16x32_bf16 v[184:187], v[148:151], v[116:119], v[184:187]\n"
    "v_mfma_f32_16x16x32_bf16 v[184:187], v[152:155], v[128:131], v[184:187]\n"
    "v_mfma_f32_16x16x32_bf16 v[184:187], v[156:159], v[140:143], v[184:187]\n"
    "s_waitcnt vmcnt(12)\n"
    "s_waitcnt lgkmcnt(0)\n"
    "s_barrier\n"
    "s_add_u32 m0, s68, 49152\n"
    "v_mfma_f32_16x16x32_bf16 v[200:203], v[160:163], v[96:99], 0\n"
    "ds_read_b128 v[144:147], v245 offset:0\n"
    "v_mfma_f32_16x16x32_bf16 v[200:203], v[164:167], v[108:111], v[200:203]\n"
    "ds_read_b128 v[148:151], v245 offset:256\n"
    "v_mfma_f32_16x16x32_bf16 v[200:203], v[168:171], v[120:123], v[200:203]\n"
    "ds_read_b128 v[152:155], v245 offset:512\n"
    "v_mfma_f32_16x16x32_bf16 v[200:203], v[172:175], v[132:135], v[200:203]\n"
    "ds_read_b128 v[156:159], v245 offset:768\n"
    "v_mfma_f32_16x16x32_bf16 v[204:207], v[160:163], v[100:103], 0\n"
    "buffer_load_dwordx4 v246, s[36:39], s70 offen offset:0 nt lds\n"
    "v_mfma_f32_16x16x32_bf16 v[204:207], v[164:167], v[112:115], v[204:207]\n"
    "buffer_load_dwordx4 v247, s[36:39], s70 offen offset:1024 nt lds\n"
    "v_mfma_f32_16x16x32_bf16 v[204:207], v[168:171], v[124:127], v[204:207]\n"
    "buffer_load_dwordx4 v246, s[36:39], s70 offen offset:2048 nt lds\n"
    "v_mfma_f32_16x16x32_bf16 v[204:207], v[172:175], v[136:139], v[204:207]\n"
    "buffer_load_dwordx4 v247, s[36:39], s70 offen offset:3072 nt lds\n"
    "v_mfma_f32_16x16x32_bf16 v[208:211], v[160:163], v[104:107], 0\n"
    "v_mfma_f32_16x16x32_bf16 v[208:211], v[164:167], v[116:119], v[208:211]\n"
    "v_mfma_f32_16x16x32_bf16 v[208:211], v[168:171], v[128:131], v[208:211]\n"
    "v_mfma_f32_16x16x32_bf16 v[208:211], v[172:175], v[140:143], v[208:211]\n"
    "s_add_u32 s70, s70, 0x2000\n"
    "v_max3_f32 v250, v176, v177, v178\n"
    "v_max3_f32 v253, v179, v200, v201\n"
    "v_max3_f32 v250, v250, v202, v203\n"
    "v_max_f32 v250, v250, v253\n"
    "v_mov_b32 v253, v250\n"
    "s_nop 1\n"
    "v_permlane16_swap_b32 v250, v253\n"
    "s_nop 0\n"
    "v_max_f32 v250, v250, v253\n"
    "v_mov_b32 v253, v250\n"
    "s_nop 1\n"
    "v_permlane32_swap_b32 v250, v253\n"
    "s_nop 0\n"
    "v_max_f32 v250, v250, v253\n"
    "v_sub_f32 v176, v176, v250\n"
    "v_sub_f32 v200, v200, v250\n"
    "v_sub_f32 v224, 0, v250\n"
    "v_sub_f32 v177, v177, v250\n"
    "v_sub_f32 v201, v201, v250\n"
    "v_sub_f32 v225, 0, v250\n"
    "v_sub_f32 v178, v178, v250\n"
    "v_sub_f32 v202, v202, v250\n"
    "v_sub_f32 v226, 0, v250\n"
    "v_sub_f32 v179, v179, v250\n"
    "v_sub_f32 v203, v203, v250\n"
    "v_sub_f32 v227, 0, v250\n"
    "v_exp_f32 v176, v176\n"
    "v_exp_f32 v200, v200\n"
    "v_exp_f32 v177, v177\n"
    "v_exp_f32 v201, v201\n"
    "v_exp_f32 v178, v178\n"
    "v_exp_f32 v202, v202\n"
    "v_exp_f32 v179, v179\n"
    "v_exp_f32 v203, v203\n"
    "v_max3_f32 v251, v180, v181, v182\n"
    "v_max3_f32 v253, v183, v204, v205\n"
    "v_max3_f32 v251, v251, v206, v207\n"
    "v_max_f32 v251, v251, v253\n"
    "v_mov_b32 v253, v251\n"
    "s_nop 1\n"
    "v_permlane16_swap_b32 v251, v253\n"
    "s_nop 0\n"
    "v_max_f32 v251, v251, v253\n"
    "v_mov_b32 v253, v251\n"
    "s_nop 1\n"
    "v_permlane32_swap_b32 v251, v253\n"
    "s_nop 0\n"
    "v_max_f32 v251, v251, v253\n"
    "v_sub_f32 v180, v180, v251\n"
    "v_sub_f32 v204, v204, v251\n"
    "v_sub_f32 v228, 0, v251\n"
    "v_sub_f32 v181, v181, v251\n"
    "v_sub_f32 v205, v205, v251\n"
    "v_sub_f32 v229, 0, v251\n"
    "v_sub_f32 v182, v182, v251\n"
    "v_sub_f32 v206, v206, v251\n"
    "v_sub_f32 v230, 0, v251\n"
    "v_sub_f32 v183, v183, v251\n"
    "v_sub_f32 v207, v207, v251\n"
    "v_sub_f32 v231, 0, v251\n"
    "v_exp_f32 v180, v180\n"
    "v_exp_f32 v204, v204\n"
    "v_exp_f32 v181, v181\n"
    "v_exp_f32 v205, v205\n"
    "v_exp_f32 v182, v182\n"
    "v_exp_f32 v206, v206\n"
    "v_exp_f32 v183, v183\n"
    "v_exp_f32 v207, v207\n"
    "v_max3_f32 v252, v184, v185, v186\n"
    "v_max3_f32 v253, v187, v208, v209\n"
    "v_max3_f32 v252, v252, v210, v211\n"
    "v_max_f32 v252, v252, v253\n"
    "v_mov_b32 v253, v252\n"
    "s_nop 1\n"
    "v_permlane16_swap_b32 v252, v253\n"
    "s_nop 0\n"
    "v_max_f32 v252, v252, v253\n"
    "v_mov_b32 v253, v252\n"
    "s_nop 1\n"
    "v_permlane32_swap_b32 v252, v253\n"
    "s_nop 0\n"
    "v_max_f32 v252, v252, v253\n"
    "v_sub_f32 v184, v184, v252\n"
    "v_sub_f32 v208, v208, v252\n"
    "v_sub_f32 v232, 0, v252\n"
    "v_sub_f32 v185, v185, v252\n"
    "v_sub_f32 v209, v209, v252\n"
    "v_sub_f32 v233, 0, v252\n"
    "v_sub_f32 v186, v186, v252\n"
    "v_sub_f32 v210, v210, v252\n"
    "v_sub_f32 v234, 0, v252\n"
    "v_sub_f32 v187, v187, v252\n"
    "v_sub_f32 v211, v211, v252\n"
    "v_sub_f32 v235, 0, v252\n"
    "v_exp_f32 v184, v184\n"
    "v_exp_f32 v208, v208\n"
    "v_exp_f32 v185, v185\n"
    "v_exp_f32 v209, v209\n"
    "v_exp_f32 v186, v186\n"
    "v_exp_f32 v210, v210\n"
    "v_exp_f32 v187, v187\n"
    "v_exp_f32 v211, v211\n"
    "v_cvt_pk_bf16_f32 v176, v176, v177\n"
    "v_cvt_pk_bf16_f32 v177, v178, v179\n"
    "v_cvt_pk_bf16_f32 v180, v180, v181\n"
    "v_cvt_pk_bf16_f32 v181, v182, v183\n"
    "v_cvt_pk_bf16_f32 v184, v184, v185\n"
    "v_cvt_pk_bf16_f32 v185, v186, v187\n"
    "v_cvt_pk_bf16_f32 v178, v200, v201\n"
    "v_cvt_pk_bf16_f32 v179, v202, v203\n"
    "v_add_u32 v245, 0xffff0000, v245\n"
    "v_add_u32 v243, 4294918144, v243\n"
    "v_add_u32 v244, 4294918144, v244\n"
    "s_mov_b32 s78, 0\n"
    "s_add_u32 m0, s68, 65536\n"
    "s_mov_b32 s76, 0\n"
    "s_mov_b32 s80, 196608\n"
    "s_cmp_eq_u32 s72, 0\n"
    "s_cselect_b32 s80, 212992, s80\n"
    "s_setprio 2\n"
    "s_cmp_eq_u32 s72, 0\n"
    "s_cbranch_scc1 Lag2%=\n"
    "s_cmp_ge_u32 s73, 0x10000000\n"
    "s_cbranch_scc1 Lag3%=\n"
    "s_add_u32 s83, s73, 8160\n"
    "s_mov_b32 s33, 0x80\n"
    "s_mov_b64 exec, 1\n"
    "Lag%=:\n"
    "buffer_load_dwordx2 v[252:253], v255, s[64:67], s83 offen sc1\n"
    "s_waitcnt vmcnt(0)\n"
    "v_cmp_eq_u64 vcc, s[88:89], v[252:253]\n"
    "s_cbranch_vccnz Lag1%=\n"
    "s_sleep 8\n"
    "s_sub_u32 s33, s33, 1\n"
    "s_cmp_lg_u32 s33, 0\n"
    "s_cbranch_scc1 Lag%=\n"
    "Lag1%=:\n"
    "s_mov_b64 exec, -1\n"
    "Lag3%=:\n"
    "s_barrier\n"
    "Lag2%=:\n"
    "s_memrealtime s[28:29]\n"
    "s_waitcnt lgkmcnt(0)\n"
    "s_cmp_eq_u32 s72, 0\n"
    "s_cselect_b32 s25, 122, 64\n"
    "s_cselect_b32 s26, 0, 58\n"
    "s_add_u32 s25, s25, s26\n"
    "s_add_u32 s24, s28, s25\n"
    "s_waitcnt vmcnt(12) lgkmcnt(0)\n"
    "s_barrier\n"
    ".p2align 6\n"
    "Lloop%=:\n"
    "v_mfma_f32_16x16x32_bf16 v[188:191], v[144:147], v[96:99], v[224:227]\n"
    "ds_read_b64_tr_b16 v[160:161], v243 offset:8192\n"
    "v_cvt_pk_bf16_f32 v182, v204, v205\n"
    "v_mfma_f32_16x16x32_bf16 v[188:191], v[148:151], v[108:111], v[188:191]\n"
    "ds_read_b64_tr_b16 v[162:163], v243 offset:12288\n"
    "v_cvt_pk_bf16_f32 v183, v206, v207\n"
    "v_mfma_f32_16x16x32_bf16 v[188:191], v[152:155], v[120:123], v[188:191]\n"
    "ds_read_b64_tr_b16 v[164:165], v244 offset:8192\n"
    "v_cvt_pk_bf16_f32 v186, v208, v209\n"
    "v_mfma_f32_16x16x32_bf16 v[188:191], v[156:159], v[132:135], v[188:191]\n"
    "ds_read_b64_tr_b16 v[166:167], v244 offset:12288\n"
    "v_cvt_pk_bf16_f32 v187, v210, v211\n"
    "v_mfma_f32_16x16x32_bf16 v[192:195], v[144:147], v[100:103], v[228:231]\n"
    "ds_read_b64_tr_b16 v[168:169], v243 offset:8448\n"
    "v_mfma_f32_16x16x32_bf16 v[192:195], v[148:151], v[112:115], v[192:195]\n"
    "ds_read_b64_tr_b16 v[170:171], v243 offset:12544\n"
    "v_mfma_f32_16x16x32_bf16 v[192:195], v[152:155], v[124:127], v[192:195]\n"
    "buffer_load_dwordx4 v246, s[36:39], s70 offen offset:0 nt lds\n"
    "v_mfma_f32_16x16x32_bf16 v[192:195], v[156:159], v[136:139], v[192:195]\n"
    "v_exp_f32 v188, v188\n"
    "v_mfma_f32_16x16x32_bf16 v[212:215], v[236:239], v[176:179], 0\n"
    "v_exp_f32 v189, v189\n"
    "v_mfma_f32_16x16x32_bf16 v[216:219], v[236:239], v[180:183], 0\n"
    "v_exp_f32 v190, v190\n"
    "v_mfma_f32_16x16x32_bf16 v[220:223], v[236:239], v[184:187], 0\n"
    "v_exp_f32 v191, v191\n"
    "v_mfma_f32_16x16x32_bf16 v[196:199], v[144:147], v[104:107], v[232:235]\n"
    "v_exp_f32 v192, v192\n"
    "v_mfma_f32_16x16x32_bf16 v[196:199], v[148:151], v[116:119], v[196:199]\n"
    "s_waitcnt lgkmcnt(0)\n"
    "v_exp_f32 v193, v193\n"
    "v_mfma_f32_16x16x32_bf16 v[196:199], v[152:155], v[128:131], v[196:199]\n"
    "ds_read_b64_tr_b16 v[172:173], v244 offset:8448\n"
    "v_max3_f32 v250, v212, v216, v220\n"
    "v_mfma_f32_16x16x32_bf16 v[196:199], v[156:159], v[140:143], v[196:199]\n"
    "ds_read_b64_tr_b16 v[174:175], v244 offset:12544\n"
    "v_cmp_lt_f32 vcc, s79, v250\n"
    "s_cbranch_vccnz Lrj0%=\n"
    "Lback0%=:\n"
    "v_mfma_f32_16x16x32_bf16 v[0:3], v[160:163], v[176:179], v[0:3]\n"
    "ds_read_b128 v[144:147], v245 offset:4096\n"
    "v_add_f32 v240, v212, v240\n"
    "v_mfma_f32_16x16x32_bf16 v[4:7], v[160:163], v[180:183], v[4:7]\n"
    "ds_read_b128 v[148:151], v245 offset:4352\n"
    "v_add_f32 v241, v216, v241\n"
    "v_mfma_f32_16x16x32_bf16 v[8:11], v[160:163], v[184:187], v[8:11]\n"
    "ds_read_b128 v[152:155], v245 offset:4608\n"
    "v_add_f32 v242, v220, v242\n"
    "v_mfma_f32_16x16x32_bf16 v[12:15], v[164:167], v[176:179], v[12:15]\n"
    "ds_read_b128 v[156:159], v245 offset:4864\n"
    "v_cvt_pk_bf16_f32 v188, v188, v189\n"
    "v_mfma_f32_16x16x32_bf16 v[16:19], v[164:167], v[180:183], v[16:19]\n"
    "buffer_load_dwordx4 v247, s[36:39], s70 offen offset:1024 nt lds\n"
    "v_mfma_f32_16x16x32_bf16 v[20:23], v[164:167], v[184:187], v[20:23]\n"
    "s_cmp_eq_u32 s78, 0x10000\n"
    "v_exp_f32 v194, v194\n"
    "v_mfma_f32_16x16x32_bf16 v[24:27], v[168:171], v[176:179], v[24:27]\n"
    "s_cselect_b32 s77, s82, s81\n"
    "v_exp_f32 v195, v195\n"
    "v_mfma_f32_16x16x32_bf16 v[28:31], v[168:171], v[180:183], v[28:31]\n"
    "s_add_u32 s76, s76, 0x2000\n"
    "v_exp_f32 v196, v196\n"
    "v_mfma_f32_16x16x32_bf16 v[32:35], v[168:171], v[184:187], v[32:35]\n"
    "s_waitcnt lgkmcnt(0)\n"
    "v_exp_f32 v197, v197\n"
    "v_mfma_f32_16x16x32_bf16 v[36:39], v[172:175], v[176:179], v[36:39]\n"
    "v_exp_f32 v198, v198\n"
    "v_mfma_f32_16x16x32_bf16 v[40:43], v[172:175], v[180:183], v[40:43]\n"
    "v_exp_f32 v199, v199\n"
    "v_mfma_f32_16x16x32_bf16 v[44:47], v[172:175], v[184:187], v[44:47]\n"
    "ds_read_b64_tr_b16 v[160:161], v243 offset:8704\n"
    "v_cvt_pk_bf16_f32 v189, v190, v191\n"
    "v_mfma_f32_16x16x32_bf16 v[200:203], v[144:147], v[96:99], v[224:227]\n"
    "ds_read_b64_tr_b16 v[162:163], v243 offset:12800\n"
    "v_cvt_pk_bf16_f32 v192, v192, v193\n"
    "v_mfma_f32_16x16x32_bf16 v[200:203], v[148:151], v[108:111], v[200:203]\n"
    "ds_read_b64_tr_b16 v[164:165], v244 offset:8704\n"
    "v_cvt_pk_bf16_f32 v193, v194, v195\n"
    "v_mfma_f32_16x16x32_bf16 v[200:203], v[152:155], v[120:123], v[200:203]\n"
    "ds_read_b64_tr_b16 v[166:167], v244 offset:12800\n"
    "v_cvt_pk_bf16_f32 v196, v196, v197\n"
    "v_mfma_f32_16x16x32_bf16 v[200:203], v[156:159], v[132:135], v[200:203]\n"
    "ds_read_b64_tr_b16 v[168:169], v243 offset:8960\n"
    "v_cvt_pk_bf16_f32 v197, v198, v199\n"
    "v_mfma_f32_16x16x32_bf16 v[204:207], v[144:147], v[100:103], v[228:231]\n"
    "ds_read_b64_tr_b16 v[170:171], v243 offset:13056\n"
    "v_mfma_f32_16x16x32_bf16 v[204:207], v[148:151], v[112:115], v[204:207]\n"
    "buffer_load_dwordx4 v246, s[36:39], s70 offen offset:2048 nt lds\n"
    "v_mfma_f32_16x16x32_bf16 v[204:207], v[152:155], v[124:127], v[204:207]\n"
    "s_nop 0\n"
    "v_exp_f32 v200, v200\n"
    "v_mfma_f32_16x16x32_bf16 v[204:207], v[156:159], v[136:139], v[204:207]\n"
    "v_exp_f32 v201, v201\n"
    "v_mfma_f32_16x16x32_bf16 v[208:211], v[144:147], v[104:107], v[232:235]\n"
    "v_exp_f32 v202, v202\n"
    "v_mfma_f32_16x16x32_bf16 v[208:211], v[148:151], v[116:119], v[208:211]\n"
    "s_waitcnt lgkmcnt(0)\n"
    "v_exp_f32 v203, v203\n"
    "v_mfma_f32_16x16x32_bf16 v[208:211], v[152:155], v[128:131], v[208:211]\n"
    "ds_read_b64_tr_b16 v[172:173], v244 offset:8960\n"
    "v_add_u32 v243, s77, v243\n"
    "v_mfma_f32_16x16x32_bf16 v[208:211], v[156:159], v[140:143], v[208:211]\n"
    "ds_read_b64_tr_b16 v[174:175], v244 offset:13056\n"
    "v_add_u32 v244, s77, v244\n"
    "v_mfma_f32_16x16x32_bf16 v[48:51], v[160:163], v[176:179], v[48:51]\n"
    "ds_read_b128 v[144:147], v245 offset:0\n"
    "v_cvt_pk_bf16_f32 v190, v200, v201\n"
    "v_mfma_f32_16x16x32_bf16 v[52:55], v[160:163], v[180:183], v[52:55]\n"
    "ds_read_b128 v[148:151], v245 offset:256\n"
    "v_cvt_pk_bf16_f32 v191, v202, v203\n"
    "v_mfma_f32_16x16x32_bf16 v[56:59], v[160:163], v[184:187], v[56:59]\n"
    "ds_read_b128 v[152:155], v245 offset:512\n"
    "v_exp_f32 v204, v204\n"
    "v_mfma_f32_16x16x32_bf16 v[60:63], v[164:167], v[176:179], v[60:63]\n"
    "ds_read_b128 v[156:159], v245 offset:768\n"
    "v_add_u32 v245, s77, v245\n"
    "v_mfma_f32_16x16x32_bf16 v[64:67], v[164:167], v[180:183], v[64:67]\n"
    "buffer_load_dwordx4 v247, s[36:39], s70 offen offset:3072 nt lds\n"
    "v_mfma_f32_16x16x32_bf16 v[68:71], v[164:167], v[184:187], v[68:71]\n"
    "s_add_u32 m0, s68, s78\n"
    "v_exp_f32 v205, v205\n"
    "v_mfma_f32_16x16x32_bf16 v[72:75], v[168:171], v[176:179], v[72:75]\n"
    "s_add_u32 s78, s78, s77\n"
    "v_exp_f32 v206, v206\n"
    "v_mfma_f32_16x16x32_bf16 v[76:79], v[168:171], v[180:183], v[76:79]\n"
    "s_add_u32 s70, s70, 0x2000\n"
    "v_exp_f32 v207, v207\n"
    "v_mfma_f32_16x16x32_bf16 v[80:83], v[168:171], v[184:187], v[80:83]\n"
    "s_waitcnt lgkmcnt(0)\n"
    "v_exp_f32 v208, v208\n"
    "v_mfma_f32_16x16x32_bf16 v[84:87], v[172:175], v[176:179], v[84:87]\n"
    "v_exp_f32 v209, v209\n"
    "v_mfma_f32_16x16x32_bf16 v[88:91], v[172:175], v[180:183], v[88:91]\n"
    "s_waitcnt vmcnt(12)\n"
    "v_exp_f32 v210, v210\n"
    "v_mfma_f32_16x16x32_bf16 v[92:95], v[172:175], v[184:187], v[92:95]\n"
    "s_barrier\n"
    "v_exp_f32 v211, v211\n"
    "v_mfma_f32_16x16x32_bf16 v[176:179], v[144:147], v[96:99], v[224:227]\n"
    "ds_read_b64_tr_b16 v[160:161], v243 offset:8192\n"
    "v_cvt_pk_bf16_f32 v194, v204, v205\n"
    "v_mfma_f32_16x16x32_bf16 v[176:179], v[148:151], v[108:111], v[176:179]\n"
    "ds_read_b64_tr_b16 v[162:163], v243 offset:12288\n"
    "v_cvt_pk_bf16_f32 v195, v206, v207\n"
    "v_mfma_f32_16x16x32_bf16 v[176:179], v[152:155], v[120:123], v[176:179]\n"
    "ds_read_b64_tr_b16 v[164:165], v244 offset:8192\n"
    "v_cvt_pk_bf16_f32 v198, v208, v209\n"
    "v_mfma_f32_16x16x32_bf16 v[176:179], v[156:159], v[132:135], v[176:179]\n"
    "ds_read_b64_tr_b16 v[166:167], v244 offset:12288\n"
    "v_cvt_pk_bf16_f32 v199, v210, v211\n"
    "v_mfma_f32_16x16x32_bf16 v[180:183], v[144:147], v[100:103], v[228:231]\n"
    "ds_read_b64_tr_b16 v[168:169], v243 offset:8448\n"
    "v_mfma_f32_16x16x32_bf16 v[180:183], v[148:151], v[112:115], v[180:183]\n"
    "ds_read_b64_tr_b16 v[170:171], v243 offset:12544\n"
    "v_mfma_f32_16x16x32_bf16 v[180:183], v[152:155], v[124:127], v[180:183]\n"
    "buffer_load_dwordx4 v246, s[36:39], s70 offen offset:0 nt lds\n"
    "v_mfma_f32_16x16x32_bf16 v[180:183], v[156:159], v[136:139], v[180:183]\n"
    "v_exp_f32 v176, v176\n"
    "v_mfma_f32_16x16x32_bf16 v[212:215], v[236:239], v[188:191], 0\n"
    "v_exp_f32 v177, v177\n"
    "v_mfma_f32_16x16x32_bf16 v[216:219], v[236:239], v[192:195], 0\n"
    "v_exp_f32 v178, v178\n"
    "v_mfma_f32_16x16x32_bf16 v[220:223], v[236:239], v[196:199], 0\n"
    "v_exp_f32 v179, v179\n"
    "v_mfma_f32_16x16x32_bf16 v[184:187], v[144:147], v[104:107], v[232:235]\n"
    "v_exp_f32 v180, v180\n"
    "v_mfma_f32_16x16x32_bf16 v[184:187], v[148:151], v[116:119], v[184:187]\n"
    "s_waitcnt lgkmcnt(0)\n"
    "v_exp_f32 v181, v181\n"
    "v_mfma_f32_16x16x32_bf16 v[184:187], v[152:155], v[128:131], v[184:187]\n"
    "ds_read_b64_tr_b16 v[172:173], v244 offset:8448\n"
    "v_max3_f32 v250, v212, v216, v220\n"
    "v_mfma_f32_16x16x32_bf16 v[184:187], v[156:159], v[140:143], v[184:187]\n"
    "ds_read_b64_tr_b16 v[174:175], v244 offset:12544\n"
    "v_cmp_lt_f32 vcc, s79, v250\n"
    "s_cbranch_vccnz Lrj1%=\n"
    "Lback1%=:\n"
    "v_mfma_f32_16x16x32_bf16 v[0:3], v[160:163], v[188:191], v[0:3]\n"
    "ds_read_b128 v[144:147], v245 offset:4096\n"
    "v_add_f32 v240, v212, v240\n"
    "v_mfma_f32_16x16x32_bf16 v[4:7], v[160:163], v[192:195], v[4:7]\n"
    "ds_read_b128 v[148:151], v245 offset:4352\n"
    "v_add_f32 v241, v216, v241\n"
    "v_mfma_f32_16x16x32_bf16 v[8:11], v[160:163], v[196:199], v[8:11]\n"
    "ds_read_b128 v[152:155], v245 offset:4608\n"
    "v_add_f32 v242, v220, v242\n"
    "v_mfma_f32_16x16x32_bf16 v[12:15], v[164:167], v[188:191], v[12:15]\n"
    "ds_read_b128 v[156:159], v245 offset:4864\n"
    "v_cvt_pk_bf16_f32 v176, v176, v177\n"
    "v_mfma_f32_16x16x32_bf16 v[16:19], v[164:167], v[192:195], v[16:19]\n"
    "buffer_load_dwordx4 v247, s[36:39], s70 offen offset:1024 nt lds\n"
    "v_mfma_f32_16x16x32_bf16 v[20:23], v[164:167], v[196:199], v[20:23]\n"
    "s_cmp_eq_u32 s78, 0x10000\n"
    "v_exp_f32 v182, v182\n"
    "v_mfma_f32_16x16x32_bf16 v[24:27], v[168:171], v[188:191], v[24:27]\n"
    "s_cselect_b32 s77, s82, s81\n"
    "v_exp_f32 v183, v183\n"
    "v_mfma_f32_16x16x32_bf16 v[28:31], v[168:171], v[192:195], v[28:31]\n"
    "s_add_u32 s76, s76, 0x2000\n"
    "v_exp_f32 v184, v184\n"
    "v_mfma_f32_16x16x32_bf16 v[32:35], v[168:171], v[196:199], v[32:35]\n"
    "s_waitcnt lgkmcnt(0)\n"
    "v_exp_f32 v185, v185\n"
    "v_mfma_f32_16x16x32_bf16 v[36:39], v[172:175], v[188:191], v[36:39]\n"
    "v_exp_f32 v186, v186\n"
    "v_mfma_f32_16x16x32_bf16 v[40:43], v[172:175], v[192:195], v[40:43]\n"
    "v_exp_f32 v187, v187\n"
    "v_mfma_f32_16x16x32_bf16 v[44:47], v[172:175], v[196:199], v[44:47]\n"
    "ds_read_b64_tr_b16 v[160:161], v243 offset:8704\n"
    "v_cvt_pk_bf16_f32 v177, v178, v179\n"
    "v_mfma_f32_16x16x32_bf16 v[200:203], v[144:147], v[96:99], v[224:227]\n"
    "ds_read_b64_tr_b16 v[162:163], v243 offset:12800\n"
    "v_cvt_pk_bf16_f32 v180, v180, v181\n"
    "v_mfma_f32_16x16x32_bf16 v[200:203], v[148:151], v[108:111], v[200:203]\n"
    "ds_read_b64_tr_b16 v[164:165], v244 offset:8704\n"
    "v_cvt_pk_bf16_f32 v181, v182, v183\n"
    "v_mfma_f32_16x16x32_bf16 v[200:203], v[152:155], v[120:123], v[200:203]\n"
    "ds_read_b64_tr_b16 v[166:167], v244 offset:12800\n"
    "v_cvt_pk_bf16_f32 v184, v184, v185\n"
    "v_mfma_f32_16x16x32_bf16 v[200:203], v[156:159], v[132:135], v[200:203]\n"
    "ds_read_b64_tr_b16 v[168:169], v243 offset:8960\n"
    "v_cvt_pk_bf16_f32 v185, v186, v187\n"
    "v_mfma_f32_16x16x32_bf16 v[204:207], v[144:147], v[100:103], v[228:231]\n"
    "ds_read_b64_tr_b16 v[170:171], v243 offset:13056\n"
    "v_mfma_f32_16x16x32_bf16 v[204:207], v[148:151], v[112:115], v[204:207]\n"
    "buffer_load_dwordx4 v246, s[36:39], s70 offen offset:2048 nt lds\n"
    "v_mfma_f32_16x16x32_bf16 v[204:207], v[152:155], v[124:127], v[204:207]\n"
    "s_nop 0\n"
    "v_exp_f32 v200, v200\n"
    "v_mfma_f32_16x16x32_bf16 v[204:207], v[156:159], v[136:139], v[204:207]\n"
    "v_exp_f32 v201, v201\n"
    "v_mfma_f32_16x16x32_bf16 v[208:211], v[144:147], v[104:107], v[232:235]\n"
    "v_exp_f32 v202, v202\n"
    "v_mfma_f32_16x16x32_bf16 v[208:211], v[148:151], v[116:119], v[208:211]\n"
    "s_waitcnt lgkmcnt(0)\n"
    "v_exp_f32 v203, v203\n"
    "v_mfma_f32_16x16x32_bf16 v[208:211], v[152:155], v[128:131], v[208:211]\n"
    "ds_read_b64_tr_b16 v[172:173], v244 offset:8960\n"
    "v_add_u32 v243, s77, v243\n"
    "v_mfma_f32_16x16x32_bf16 v[208:211], v[156:159], v[140:143], v[208:211]\n"
    "ds_read_b64_tr_b16 v[174:175], v244 offset:13056\n"
    "v_add_u32 v244, s77, v244\n"
    "v_mfma_f32_16x16x32_bf16 v[48:51], v[160:163], v[188:191], v[48:51]\n"
    "ds_read_b128 v[144:147], v245 offset:0\n"
    "v_cvt_pk_bf16_f32 v178, v200, v201\n"
    "v_mfma_f32_16x16x32_bf16 v[52:55], v[160:163], v[192:195], v[52:55]\n"
    "ds_read_b128 v[148:151], v245 offset:256\n"
    "v_cvt_pk_bf16_f32 v179, v202, v203\n"
    "v_mfma_f32_16x16x32_bf16 v[56:59], v[160:163], v[196:199], v[56:59]\n"
    "ds_read_b128 v[152:155], v245 offset:512\n"
    "v_exp_f32 v204, v204\n"
    "v_mfma_f32_16x16x32_bf16 v[60:63], v[164:167], v[188:191], v[60:63]\n"
    "ds_read_b128 v[156:159], v245 offset:768\n"
    "v_add_u32 v245, s77, v245\n"
    "v_mfma_f32_16x16x32_bf16 v[64:67], v[164:167], v[192:195], v[64:67]\n"
    "buffer_load_dwordx4 v247, s[36:39], s70 offen offset:3072 nt lds\n"
    "v_mfma_f32_16x16x32_bf16 v[68:71], v[164:167], v[196:199], v[68:71]\n"
    "s_add_u32 m0, s68, s78\n"
    "v_exp_f32 v205, v205\n"
    "v_mfma_f32_16x16x32_bf16 v[72:75], v[168:171], v[188:191], v[72:75]\n"
    "s_add_u32 s78, s78, s77\n"
    "v_exp_f32 v206, v206\n"
    "v_mfma_f32_16x16x32_bf16 v[76:79], v[168:171], v[192:195], v[76:79]\n"
    "s_add_u32 s70, s70, 0x2000\n"
    "s_memrealtime s[28:29]\n"
    "v_exp_f32 v207, v207\n"
    "v_mfma_f32_16x16x32_bf16 v[80:83], v[168:171], v[196:199], v[80:83]\n"
    "s_waitcnt lgkmcnt(0)\n"
    "v_exp_f32 v208, v208\n"
    "v_mfma_f32_16x16x32_bf16 v[84:87], v[172:175], v[188:191], v[84:87]\n"
    "s_sub_u32 s26, s24, s28\n"
    "s_max_i32 s26, s26, 0\n"
    "s_add_u32 s24, s28, s26\n"
    "s_add_u32 s24, s24, s25\n"
    "s_min_i32 s26, s26, 1000\n"
    "s_cmp_lt_i32 s26, 3\n"
    "s_cbranch_scc1 Lpc0b%=\n"
    "Lpc0a%=:\n"
    "s_sleep 1\n"
    "s_sub_u32 s26, s26, 4\n"
    "s_cmp_gt_i32 s26, 2\n"
    "s_cbranch_scc1 Lpc0a%=\n"
    "Lpc0b%=:\n"
    "s_cmp_lg_u32 s76, s80\n"
    "v_exp_f32 v209, v209\n"
    "v_mfma_f32_16x16x32_bf16 v[88:91], v[172:175], v[192:195], v[88:91]\n"
    "s_waitcnt vmcnt(12)\n"
    "v_exp_f32 v210, v210\n"
    "v_mfma_f32_16x16x32_bf16 v[92:95], v[172:175], v[196:199], v[92:95]\n"
    "s_barrier\n"
    "v_exp_f32 v211, v211\n"
    "s_cbranch_scc1 Lloop%=\n"
    "s_cmp_eq_u32 s80, 229376\n"
    "s_cbranch_scc1 Ltail%=\n"
    "s_mov_b32 s80, 229376\n"
    "v_mov_b32 v252, s88\n"
    "v_mov_b32 v253, s89\n"
    "s_add_u32 s83, s73, 0x2000\n"
    "s_mov_b64 exec, 1\n"
    "buffer_store_dwordx2 v[252:253], v255, s[64:67], s83 offen sc1\n"
    "s_mov_b64 exec, -1\n"
    "s_branch Lloop%=\n"
    "Ltail%=:\n"
    "v_mfma_f32_16x16x32_bf16 v[188:191], v[144:147], v[96:99], v[224:227]\n"
    "ds_read_b64_tr_b16 v[160:161], v243 offset:8192\n"
    "v_cvt_pk_bf16_f32 v182, v204, v205\n"
    "v_mfma_f32_16x16x32_bf16 v[188:191], v[148:151], v[108:111], v[188:191]\n"
    "ds_read_b64_tr_b16 v[162:163], v243 offset:12288\n"
    "v_cvt_pk_bf16_f32 v183, v206, v207\n"
    "v_mfma_f32_16x16x32_bf16 v[188:191], v[152:155], v[120:123], v[188:191]\n"
    "ds_read_b64_tr_b16 v[164:165], v244 offset:8192\n"
    "v_cvt_pk_bf16_f32 v186, v208, v209\n"
    "v_mfma_f32_16x16x32_bf16 v[188:191], v[156:159], v[132:135], v[188:191]\n"
    "ds_read_b64_tr_b16 v[166:167], v244 offset:12288\n"
    "v_cvt_pk_bf16_f32 v187, v210, v211\n"
    "v_mfma_f32_16x16x32_bf16 v[192:195], v[144:147], v[100:103], v[228:231]\n"
    "ds_read_b64_tr_b16 v[168:169], v243 offset:8448\n"
    "v_mfma_f32_16x16x32_bf16 v[192:195], v[148:151], v[112:115], v[192:195]\n"
    "ds_read_b64_tr_b16 v[170:171], v243 offset:12544\n"
    "v_mfma_f32_16x16x32_bf16 v[192:195], v[152:155], v[124:127], v[192:195]\n"
    "v_mfma_f32_16x16x32_bf16 v[192:195], v[156:159], v[136:139], v[192:195]\n"
    "v_exp_f32 v188, v188\n"
    "v_mfma_f32_16x16x32_bf16 v[212:215], v[236:239], v[176:179], 0\n"
    "v_exp_f32 v189, v189\n"
    "v_mfma_f32_16x16x32_bf16 v[216:219], v[236:239], v[180:183], 0\n"
    "v_exp_f32 v190, v190\n"
    "v_mfma_f32_16x16x32_bf16 v[220:223], v[236:239], v[184:187], 0\n"
    "v_exp_f32 v191, v191\n"
    "v_mfma_f32_16x16x32_bf16 v[196:199], v[144:147], v[104:107], v[232:235]\n"
    "v_exp_f32 v192, v192\n"
    "v_mfma_f32_16x16x32_bf16 v[196:199], v[148:151], v[116:119], v[196:199]\n"
    "s_waitcnt lgkmcnt(0)\n"
    "v_exp_f32 v193, v193\n"
    "v_mfma_f32_16x16x32_bf16 v[196:199], v[152:155], v[128:131], v[196:199]\n"
    "ds_read_b64_tr_b16 v[172:173], v244 offset:8448\n"
    "v_max3_f32 v250, v212, v216, v220\n"
    "v_mfma_f32_16x16x32_bf16 v[196:199], v[156:159], v[140:143], v[196:199]\n"
    "ds_read_b64_tr_b16 v[174:175], v244 offset:12544\n"
    "v_cmp_lt_f32 vcc, s79, v250\n"
    "s_cbranch_vccnz Lrj2%=\n"
    "Lback2%=:\n"
    "v_mfma_f32_16x16x32_bf16 v[0:3], v[160:163], v[176:179], v[0:3]\n"
    "ds_read_b128 v[144:147], v245 offset:4096\n"
    "v_add_f32 v240, v212, v240\n"
    "v_mfma_f32_16x16x32_bf16 v[4:7], v[160:163], v[180:183], v[4:7]\n"
    "ds_read_b128 v[148:151], v245 offset:4352\n"
    "v_add_f32 v241, v216, v241\n"
    "v_mfma_f32_16x16x32_bf16 v[8:11], v[160:163], v[184:187], v[8:11]\n"
    "ds_read_b128 v[152:155], v245 offset:4608\n"
    "v_add_f32 v242, v220, v242\n"
    "v_mfma_f32_16x16x32_bf16 v[12:15], v[164:167], v[176:179], v[12:15]\n"
    "ds_read_b128 v[156:159], v245 offset:4864\n"
    "v_cvt_pk_bf16_f32 v188, v188, v189\n"
    "v_mfma_f32_16x16x32_bf16 v[16:19], v[164:167], v[180:183], v[16:19]\n"
    "v_mfma_f32_16x16x32_bf16 v[20:23], v[164:167], v[184:187], v[20:23]\n"
    "s_cmp_eq_u32 s78, 0x10000\n"
    "v_exp_f32 v194, v194\n"
    "v_mfma_f32_16x16x32_bf16 v[24:27], v[168:171], v[176:179], v[24:27]\n"
    "s_cselect_b32 s77, s82, s81\n"
    "v_exp_f32 v195, v195\n"
    "v_mfma_f32_16x16x32_bf16 v[28:31], v[168:171], v[180:183], v[28:31]\n"
    "s_add_u32 s76, s76, 0x2000\n"
    "v_exp_f32 v196, v196\n"
    "v_mfma_f32_16x16x32_bf16 v[32:35], v[168:171], v[184:187], v[32:35]\n"
    "s_waitcnt lgkmcnt(0)\n"
    "v_exp_f32 v197, v197\n"
    "v_mfma_f32_16x16x32_bf16 v[36:39], v[172:175], v[176:179], v[36:39]\n"
    "v_exp_f32 v198, v198\n"
    "v_mfma_f32_16x16x32_bf16 v[40:43], v[172:175], v[180:183], v[40:43]\n"
    "v_exp_f32 v199, v199\n"
    "v_mfma_f32_16x16x32_bf16 v[44:47], v[172:175], v[184:187], v[44:47]\n"
    "ds_read_b64_tr_b16 v[160:161], v243 offset:8704\n"
    "v_cvt_pk_bf16_f32 v189, v190, v191\n"
    "v_mfma_f32_16x16x32_bf16 v[200:203], v[144:147], v[96:99], v[224:227]\n"
    "ds_read_b64_tr_b16 v[162:163], v243 offset:12800\n"
    "v_cvt_pk_bf16_f32 v192, v192, v193\n"
    "v_mfma_f32_16x16x32_bf16 v[200:203], v[148:151], v[108:111], v[200:203]\n"
    "ds_read_b64_tr_b16 v[164:165], v244 offset:8704\n"
    "v_cvt_pk_bf16_f32 v193, v194, v195\n"
    "v_mfma_f32_16x16x32_bf16 v[200:203], v[152:155], v[120:123], v[200:203]\n"
    "ds_read_b64_tr_b16 v[166:167], v244 offset:12800\n"
    "v_cvt_pk_bf16_f32 v196, v196, v197\n"
    "v_mfma_f32_16x16x32_bf16 v[200:203], v[156:159], v[132:135], v[200:203]\n"
    "ds_read_b64_tr_b16 v[168:169], v243 offset:8960\n"
    "v_cvt_pk_bf16_f32 v197, v198, v199\n"
    "v_mfma_f32_16x16x32_bf16 v[204:207], v[144:147], v[100:103], v[228:231]\n"
    "ds_read_b64_tr_b16 v[170:171], v243 offset:13056\n"
    "v_mfma_f32_16x16x32_bf16 v[204:207], v[148:151], v[112:115], v[204:207]\n"
    "v_mfma_f32_16x16x32_bf16 v[204:207], v[152:155], v[124:127], v[204:207]\n"
    "s_nop 1\n"
    "v_exp_f32 v200, v200\n"
    "v_mfma_f32_16x16x32_bf16 v[204:207], v[156:159], v[136:139], v[204:207]\n"
    "v_exp_f32 v201, v201\n"
    "v_mfma_f32_16x16x32_bf16 v[208:211], v[144:147], v[104:107], v[232:235]\n"
    "v_exp_f32 v202, v202\n"
    "v_mfma_f32_16x16x32_bf16 v[208:211], v[148:151], v[116:119], v[208:211]\n"
    "s_waitcnt lgkmcnt(0)\n"
    "v_exp_f32 v203, v203\n"
    "v_mfma_f32_16x16x32_bf16 v[208:211], v[152:155], v[128:131], v[208:211]\n"
    "ds_read_b64_tr_b16 v[172:173], v244 offset:8960\n"
    "v_add_u32 v243, s77, v243\n"
    "v_mfma_f32_16x16x32_bf16 v[208:211], v[156:159], v[140:143], v[208:211]\n"
    "ds_read_b64_tr_b16 v[174:175], v244 offset:13056\n"
    "v_add_u32 v244, s77, v244\n"
    "v_mfma_f32_16x16x32_bf16 v[48:51], v[160:163], v[176:179], v[48:51]\n"
    "ds_read_b128 v[144:147], v245 offset:0\n"
    "v_cvt_pk_bf16_f32 v190, v200, v201\n"
    "v_mfma_f32_16x16x32_bf16 v[52:55], v[160:163], v[180:183], v[52:55]\n"
    "ds_read_b128 v[148:151], v245 offset:256\n"
    "v_cvt_pk_bf16_f32 v191, v202, v203\n"
    "v_mfma_f32_16x16x32_bf16 v[56:59], v[160:163], v[184:187], v[56:59]\n"
    "ds_read_b128 v[152:155], v245 offset:512\n"
    "v_exp_f32 v204, v204\n"
    "v_mfma_f32_16x16x32_bf16 v[60:63], v[164:167], v[176:179], v[60:63]\n"
    "ds_read_b128 v[156:159], v245 offset:768\n"
    "v_add_u32 v245, s77, v245\n"
    "v_mfma_f32_16x16x32_bf16 v[64:67], v[164:167], v[180:183], v[64:67]\n"
    "v_mfma_f32_16x16x32_bf16 v[68:71], v[164:167], v[184:187], v[68:71]\n"
    "s_add_u32 m0, s68, s78\n"
    "v_exp_f32 v205, v205\n"
    "v_mfma_f32_16x16x32_bf16 v[72:75], v[168:171], v[176:179], v[72:75]\n"
    "s_add_u32 s78, s78, s77\n"
    "v_exp_f32 v206, v206\n"
    "v_mfma_f32_16x16x32_bf16 v[76:79], v[168:171], v[180:183], v[76:79]\n"
    "s_add_u32 s70, s70, 0x2000\n"
    "v_exp_f32 v207, v207\n"
    "v_mfma_f32_16x16x32_bf16 v[80:83], v[168:171], v[184:187], v[80:83]\n"
    "s_waitcnt lgkmcnt(0)\n"
    "v_exp_f32 v208, v208\n"
    "v_mfma_f32_16x16x32_bf16 v[84:87], v[172:175], v[176:179], v[84:87]\n"
    "v_exp_f32 v209, v209\n"
    "v_mfma_f32_16x16x32_bf16 v[88:91], v[172:175], v[180:183], v[88:91]\n"
    "s_waitcnt vmcnt(8)\n"
    "v_exp_f32 v210, v210\n"
    "v_mfma_f32_16x16x32_bf16 v[92:95], v[172:175], v[184:187], v[92:95]\n"
    "s_barrier\n"
    "v_exp_f32 v211, v211\n"
    "v_mfma_f32_16x16x32_bf16 v[176:179], v[144:147], v[96:99], v[224:227]\n"
    "ds_read_b64_tr_b16 v[160:161], v243 offset:8192\n"
    "v_cvt_pk_bf16_f32 v194, v204, v205\n"
    "v_mfma_f32_16x16x32_bf16 v[176:179], v[148:151], v[108:111], v[176:179]\n"
    "ds_read_b64_tr_b16 v[162:163], v243 offset:12288\n"
    "v_cvt_pk_bf16_f32 v195, v206, v207\n"
    "v_mfma_f32_16x16x32_bf16 v[176:179], v[152:155], v[120:123], v[176:179]\n"
    "ds_read_b64_tr_b16 v[164:165], v244 offset:8192\n"
    "v_cvt_pk_bf16_f32 v198, v208, v209\n"
    "v_mfma_f32_16x16x32_bf16 v[176:179], v[156:159], v[132:135], v[176:179]\n"
    "ds_read_b64_tr_b16 v[166:167], v244 offset:12288\n"
    "v_cvt_pk_bf16_f32 v199, v210, v211\n"
    "v_mfma_f32_16x16x32_bf16 v[180:183], v[144:147], v[100:103], v[228:231]\n"
    "ds_read_b64_tr_b16 v[168:169], v243 offset:8448\n"
    "v_mfma_f32_16x16x32_bf16 v[180:183], v[148:151], v[112:115], v[180:183]\n"
    "ds_read_b64_tr_b16 v[170:171], v243 offset:12544\n"
    "v_mfma_f32_16x16x32_bf16 v[180:183], v[152:155], v[124:127], v[180:183]\n"
    "v_mfma_f32_16x16x32_bf16 v[180:183], v[156:159], v[136:139], v[180:183]\n"
    "v_exp_f32 v176, v176\n"
    "v_mfma_f32_16x16x32_bf16 v[212:215], v[236:239], v[188:191], 0\n"
    "v_exp_f32 v177, v177\n"
    "v_mfma_f32_16x16x32_bf16 v[216:219], v[236:239], v[192:195], 0\n"
    "v_exp_f32 v178, v178\n"
    "v_mfma_f32_16x16x32_bf16 v[220:223], v[236:239], v[196:199], 0\n"
    "v_exp_f32 v179, v179\n"
    "v_mfma_f32_16x16x32_bf16 v[184:187], v[144:147], v[104:107], v[232:235]\n"
    "v_exp_f32 v180, v180\n"
    "v_mfma_f32_16x16x32_bf16 v[184:187], v[148:151], v[116:119], v[184:187]\n"
    "s_waitcnt lgkmcnt(0)\n"
    "v_exp_f32 v181, v181\n"
    "v_mfma_f32_16x16x32_bf16 v[184:187], v[152:155], v[128:131], v[184:187]\n"
    "ds_read_b64_tr_b16 v[172:173], v244 offset:8448\n"
    "v_max3_f32 v250, v212, v216, v220\n"
    "v_mfma_f32_16x16x32_bf16 v[184:187], v[156:159], v[140:143], v[184:187]\n"
    "ds_read_b64_tr_b16 v[174:175], v244 offset:12544\n"
    "v_cmp_lt_f32 vcc, s79, v250\n"
    "s_cbranch_vccnz Lrj3%=\n"
    "Lback3%=:\n"
    "v_mfma_f32_16x16x32_bf16 v[0:3], v[160:163], v[188:191], v[0:3]\n"
    "ds_read_b128 v[144:147], v245 offset:4096\n"
    "v_add_f32 v240, v212, v240\n"
    "v_mfma_f32_16x16x32_bf16 v[4:7], v[160:163], v[192:195], v[4:7]\n"
    "ds_read_b128 v[148:151], v245 offset:4352\n"
    "v_add_f32 v241, v216, v241\n"
    "v_mfma_f32_16x16x32_bf16 v[8:11], v[160:163], v[196:199], v[8:11]\n"
    "ds_read_b128 v[152:155], v245 offset:4608\n"
    "v_add_f32 v242, v220, v242\n"
    "v_mfma_f32_16x16x32_bf16 v[12:15], v[164:167], v[188:191], v[12:15]\n"
    "ds_read_b128 v[156:159], v245 offset:4864\n"
    "v_cvt_pk_bf16_f32 v176, v176, v177\n"
    "v_mfma_f32_16x16x32_bf16 v[16:19], v[164:167], v[192:195], v[16:19]\n"
    "v_mfma_f32_16x16x32_bf16 v[20:23], v[164:167], v[196:199], v[20:23]\n"
    "s_cmp_eq_u32 s78, 0x10000\n"
    "v_exp_f32 v182, v182\n"
    "v_mfma_f32_16x16x32_bf16 v[24:27], v[168:171], v[188:191], v[24:27]\n"
    "s_cselect_b32 s77, s82, s81\n"
    "v_exp_f32 v183, v183\n"
    "v_mfma_f32_16x16x32_bf16 v[28:31], v[168:171], v[192:195], v[28:31]\n"
    "s_add_u32 s76, s76, 0x2000\n"
    "v_exp_f32 v184, v184\n"
    "v_mfma_f32_16x16x32_bf16 v[32:35], v[168:171], v[196:199], v[32:35]\n"
    "s_waitcnt lgkmcnt(0)\n"
    "v_exp_f32 v185, v185\n"
    "v_mfma_f32_16x16x32_bf16 v[36:39], v[172:175], v[188:191], v[36:39]\n"
    "v_exp_f32 v186, v186\n"
    "v_mfma_f32_16x16x32_bf16 v[40:43], v[172:175], v[192:195], v[40:43]\n"
    "v_exp_f32 v187, v187\n"
    "v_mfma_f32_16x16x32_bf16 v[44:47], v[172:175], v[196:199], v[44:47]\n"
    "ds_read_b64_tr_b16 v[160:161], v243 offset:8704\n"
    "v_cvt_pk_bf16_f32 v177, v178, v179\n"
    "v_mfma_f32_16x16x32_bf16 v[200:203], v[144:147], v[96:99], v[224:227]\n"
    "ds_read_b64_tr_b16 v[162:163], v243 offset:12800\n"
    "v_cvt_pk_bf16_f32 v180, v180, v181\n"
    "v_mfma_f32_16x16x32_bf16 v[200:203], v[148:151], v[108:111], v[200:203]\n"
    "ds_read_b64_tr_b16 v[164:165], v244 offset:8704\n"
    "v_cvt_pk_bf16_f32 v181, v182, v183\n"
    "v_mfma_f32_16x16x32_bf16 v[200:203], v[152:155], v[120:123], v[200:203]\n"
    "ds_read_b64_tr_b16 v[166:167], v244 offset:12800\n"
    "v_cvt_pk_bf16_f32 v184, v184, v185\n"
    "v_mfma_f32_16x16x32_bf16 v[200:203], v[156:159], v[132:135], v[200:203]\n"
    "ds_read_b64_tr_b16 v[168:169], v243 offset:8960\n"
    "v_cvt_pk_bf16_f32 v185, v186, v187\n"
    "v_mfma_f32_16x16x32_bf16 v[204:207], v[144:147], v[100:103], v[228:231]\n"
    "ds_read_b64_tr_b16 v[170:171], v243 offset:13056\n"
    "v_mfma_f32_16x16x32_bf16 v[204:207], v[148:151], v[112:115], v[204:207]\n"
    "v_mfma_f32_16x16x32_bf16 v[204:207], v[152:155], v[124:127], v[204:207]\n"
    "s_nop 1\n"
    "v_exp_f32 v200, v200\n"
    "v_mfma_f32_16x16x32_bf16 v[204:207], v[156:159], v[136:139], v[204:207]\n"
    "v_exp_f32 v201, v201\n"
    "v_mfma_f32_16x16x32_bf16 v[208:211], v[144:147], v[104:107], v[232:235]\n"
    "v_exp_f32 v202, v202\n"
    "v_mfma_f32_16x16x32_bf16 v[208:211], v[148:151], v[116:119], v[208:211]\n"
    "s_waitcnt lgkmcnt(0)\n"
    "v_exp_f32 v203, v203\n"
    "v_mfma_f32_16x16x32_bf16 v[208:211], v[152:155], v[128:131], v[208:211]\n"
    "ds_read_b64_tr_b16 v[172:173], v244 offset:8960\n"
    "v_add_u32 v243, s77, v243\n"
    "v_mfma_f32_16x16x32_bf16 v[208:211], v[156:159], v[140:143], v[208:211]\n"
    "ds_read_b64_tr_b16 v[174:175], v244 offset:13056\n"
    "v_add_u32 v244, s77, v244\n"
    "v_mfma_f32_16x16x32_bf16 v[48:51], v[160:163], v[188:191], v[48:51]\n"
    "ds_read_b128 v[144:147], v245 offset:0\n"
    "v_cvt_pk_bf16_f32 v178, v200, v201\n"
    "v_mfma_f32_16x16x32_bf16 v[52:55], v[160:163], v[192:195], v[52:55]\n"
    "ds_read_b128 v[148:151], v245 offset:256\n"
    "v_cvt_pk_bf16_f32 v179, v202, v203\n"
    "v_mfma_f32_16x16x32_bf16 v[56:59], v[160:163], v[196:199], v[56:59]\n"
    "ds_read_b128 v[152:155], v245 offset:512\n"
    "v_exp_f32 v204, v204\n"
    "v_mfma_f32_16x16x32_bf16 v[60:63], v[164:167], v[188:191], v[60:63]\n"
    "ds_read_b128 v[156:159], v245 offset:768\n"
    "v_add_u32 v245, s77, v245\n"
    "v_mfma_f32_16x16x32_bf16 v[64:67], v[164:167], v[192:195], v[64:67]\n"
    "v_mfma_f32_16x16x32_bf16 v[68:71], v[164:167], v[196:199], v[68:71]\n"
    "s_add_u32 m0, s68, s78\n"
    "v_exp_f32 v205, v205\n"
    "v_mfma_f32_16x16x32_bf16 v[72:75], v[168:171], v[188:191], v[72:75]\n"
    "s_add_u32 s78, s78, s77\n"
    "v_exp_f32 v206, v206\n"
    "v_mfma_f32_16x16x32_bf16 v[76:79], v[168:171], v[192:195], v[76:79]\n"
    "s_add_u32 s70, s70, 0x2000\n"
    "v_exp_f32 v207, v207\n"
    "v_mfma_f32_16x16x32_bf16 v[80:83], v[168:171], v[196:199], v[80:83]\n"
    "s_waitcnt lgkmcnt(0)\n"
    "v_exp_f32 v208, v208\n"
    "v_mfma_f32_16x16x32_bf16 v[84:87], v[172:175], v[188:191], v[84:87]\n"
    "v_exp_f32 v209, v209\n"
    "v_mfma_f32_16x16x32_bf16 v[88:91], v[172:175], v[192:195], v[88:91]\n"
    "s_waitcnt vmcnt(4)\n"
    "v_exp_f32 v210, v210\n"
    "v_mfma_f32_16x16x32_bf16 v[92:95], v[172:175], v[196:199], v[92:95]\n"
    "s_barrier\n"
    "v_exp_f32 v211, v211\n"
    "v_mfma_f32_16x16x32_bf16 v[188:191], v[144:147], v[96:99], v[224:227]\n"
    "ds_read_b64_tr_b16 v[160:161], v243 offset:8192\n"
    "v_cvt_pk_bf16_f32 v182, v204, v205\n"
    "v_mfma_f32_16x16x32_bf16 v[188:191], v[148:151], v[108:111], v[188:191]\n"
    "ds_read_b64_tr_b16 v[162:163], v243 offset:12288\n"
    "v_cvt_pk_bf16_f32 v183, v206, v207\n"
    "v_mfma_f32_16x16x32_bf16 v[188:191], v[152:155], v[120:123], v[188:191]\n"
    "ds_read_b64_tr_b16 v[164:165], v244 offset:8192\n"
    "v_cvt_pk_bf16_f32 v186, v208, v209\n"
    "v_mfma_f32_16x16x32_bf16 v[188:191], v[156:159], v[132:135], v[188:191]\n"
    "ds_read_b64_tr_b16 v[166:167], v244 offset:12288\n"
    "v_cvt_pk_bf16_f32 v187, v210, v211\n"
    "v_mfma_f32_16x16x32_bf16 v[192:195], v[144:147], v[100:103], v[228:231]\n"
    "ds_read_b64_tr_b16 v[168:169], v243 offset:8448\n"
    "v_mfma_f32_16x16x32_bf16 v[192:195], v[148:151], v[112:115], v[192:195]\n"
    "ds_read_b64_tr_b16 v[170:171], v243 offset:12544\n"
    "v_mfma_f32_16x16x32_bf16 v[192:195], v[152:155], v[124:127], v[192:195]\n"
    "v_mfma_f32_16x16x32_bf16 v[192:195], v[156:159], v[136:139], v[192:195]\n"
    "v_exp_f32 v188, v188\n"
    "v_mfma_f32_16x16x32_bf16 v[212:215], v[236:239], v[176:179], 0\n"
    "v_exp_f32 v189, v189\n"
    "v_mfma_f32_16x16x32_bf16 v[216:219], v[236:239], v[180:183], 0\n"
    "v_exp_f32 v190, v190\n"
    "v_mfma_f32_16x16x32_bf16 v[220:223], v[236:239], v[184:187], 0\n"
    "v_exp_f32 v191, v191\n"
    "v_mfma_f32_16x16x32_bf16 v[196:199], v[144:147], v[104:107], v[232:235]\n"
    "v_exp_f32 v192, v192\n"
    "v_mfma_f32_16x16x32_bf16 v[196:199], v[148:151], v[116:119], v[196:199]\n"
    "s_waitcnt lgkmcnt(0)\n"
    "v_exp_f32 v193, v193\n"
    "v_mfma_f32_16x16x32_bf16 v[196:199], v[152:155], v[128:131], v[196:199]\n"
    "ds_read_b64_tr_b16 v[172:173], v244 offset:8448\n"
    "v_max3_f32 v250, v212, v216, v220\n"
    "v_mfma_f32_16x16x32_bf16 v[196:199], v[156:159], v[140:143], v[196:199]\n"
    "ds_read_b64_tr_b16 v[174:175], v244 offset:12544\n"
    "v_cmp_lt_f32 vcc, s79, v250\n"
    "s_cbranch_vccnz Lrj4%=\n"
    "Lback4%=:\n"
    "v_mfma_f32_16x16x32_bf16 v[0:3], v[160:163], v[176:179], v[0:3]\n"
    "ds_read_b128 v[144:147], v245 offset:4096\n"
    "v_add_f32 v240, v212, v240\n"
    "v_mfma_f32_16x16x32_bf16 v[4:7], v[160:163], v[180:183], v[4:7]\n"
    "ds_read_b128 v[148:151], v245 offset:4352\n"
    "v_add_f32 v241, v216, v241\n"
    "v_mfma_f32_16x16x32_bf16 v[8:11], v[160:163], v[184:187], v[8:11]\n"
    "ds_read_b128 v[152:155], v245 offset:4608\n"
    "v_add_f32 v242, v220, v242\n"
    "v_mfma_f32_16x16x32_bf16 v[12:15], v[164:167], v[176:179], v[12:15]\n"
    "ds_read_b128 v[156:159], v245 offset:4864\n"
    "v_cvt_pk_bf16_f32 v188, v188, v189\n"
    "v_mfma_f32_16x16x32_bf16 v[16:19], v[164:167], v[180:183], v[16:19]\n"
    "v_mfma_f32_16x16x32_bf16 v[20:23], v[164:167], v[184:187], v[20:23]\n"
    "s_cmp_eq_u32 s78, 0x10000\n"
    "v_exp_f32 v194, v194\n"
    "v_mfma_f32_16x16x32_bf16 v[24:27], v[168:171], v[176:179], v[24:27]\n"
    "s_cselect_b32 s77, s82, s81\n"
    "v_exp_f32 v195, v195\n"
    "v_mfma_f32_16x16x32_bf16 v[28:31], v[168:171], v[180:183], v[28:31]\n"
    "s_add_u32 s76, s76, 0x2000\n"
    "v_exp_f32 v196, v196\n"
    "v_mfma_f32_16x16x32_bf16 v[32:35], v[168:171], v[184:187], v[32:35]\n"
    "s_waitcnt lgkmcnt(0)\n"
    "v_exp_f32 v197, v197\n"
    "v_mfma_f32_16x16x32_bf16 v[36:39], v[172:175], v[176:179], v[36:39]\n"
    "v_exp_f32 v198, v198\n"
    "v_mfma_f32_16x16x32_bf16 v[40:43], v[172:175], v[180:183], v[40:43]\n"
    "v_exp_f32 v199, v199\n"
    "v_mfma_f32_16x16x32_bf16 v[44:47], v[172:175], v[184:187], v[44:47]\n"
    "ds_read_b64_tr_b16 v[160:161], v243 offset:8704\n"
    "v_cvt_pk_bf16_f32 v189, v190, v191\n"
    "v_mfma_f32_16x16x32_bf16 v[200:203], v[144:147], v[96:99], v[224:227]\n"
    "ds_read_b64_tr_b16 v[162:163], v243 offset:12800\n"
    "v_cvt_pk_bf16_f32 v192, v192, v193\n"
    "v_mfma_f32_16x16x32_bf16 v[200:203], v[148:151], v[108:111], v[200:203]\n"
    "ds_read_b64_tr_b16 v[164:165], v244 offset:8704\n"
    "v_cvt_pk_bf16_f32 v193, v194, v195\n"
    "v_mfma_f32_16x16x32_bf16 v[200:203], v[152:155], v[120:123], v[200:203]\n"
    "ds_read_b64_tr_b16 v[166:167], v244 offset:12800\n"
    "v_cvt_pk_bf16_f32 v196, v196, v197\n"
    "v_mfma_f32_16x16x32_bf16 v[200:203], v[156:159], v[132:135], v[200:203]\n"
    "ds_read_b64_tr_b16 v[168:169], v243 offset:8960\n"
    "v_cvt_pk_bf16_f32 v197, v198, v199\n"
    "v_mfma_f32_16x16x32_bf16 v[204:207], v[144:147], v[100:103], v[228:231]\n"
    "ds_read_b64_tr_b16 v[170:171], v243 offset:13056\n"
    "v_mfma_f32_16x16x32_bf16 v[204:207], v[148:151], v[112:115], v[204:207]\n"
    "v_mfma_f32_16x16x32_bf16 v[204:207], v[152:155], v[124:127], v[204:207]\n"
    "s_nop 1\n"
    "v_exp_f32 v200, v200\n"
    "v_mfma_f32_16x16x32_bf16 v[204:207], v[156:159], v[136:139], v[204:207]\n"
    "v_exp_f32 v201, v201\n"
    "v_mfma_f32_16x16x32_bf16 v[208:211], v[144:147], v[104:107], v[232:235]\n"
    "v_exp_f32 v202, v202\n"
    "v_mfma_f32_16x16x32_bf16 v[208:211], v[148:151], v[116:119], v[208:211]\n"
    "s_waitcnt lgkmcnt(0)\n"
    "v_exp_f32 v203, v203\n"
    "v_mfma_f32_16x16x32_bf16 v[208:211], v[152:155], v[128:131], v[208:211]\n"
    "ds_read_b64_tr_b16 v[172:173], v244 offset:8960\n"
    "v_add_u32 v243, s77, v243\n"
    "v_mfma_f32_16x16x32_bf16 v[208:211], v[156:159], v[140:143], v[208:211]\n"
    "ds_read_b64_tr_b16 v[174:175], v244 offset:13056\n"
    "v_add_u32 v244, s77, v244\n"
    "v_mfma_f32_16x16x32_bf16 v[48:51], v[160:163], v[176:179], v[48:51]\n"
    "ds_read_b128 v[144:147], v245 offset:0\n"
    "v_cvt_pk_bf16_f32 v190, v200, v201\n"
    "v_mfma_f32_16x16x32_bf16 v[52:55], v[160:163], v[180:183], v[52:55]\n"
    "ds_read_b128 v[148:151], v245 offset:256\n"
    "v_cvt_pk_bf16_f32 v191, v202, v203\n"
    "v_mfma_f32_16x16x32_bf16 v[56:59], v[160:163], v[184:187], v[56:59]\n"
    "ds_read_b128 v[152:155], v245 offset:512\n"
    "v_exp_f32 v204, v204\n"
    "v_mfma_f32_16x16x32_bf16 v[60:63], v[164:167], v[176:179], v[60:63]\n"
    "ds_read_b128 v[156:159], v245 offset:768\n"
    "v_add_u32 v245, s77, v245\n"
    "v_mfma_f32_16x16x32_bf16 v[64:67], v[164:167], v[180:183], v[64:67]\n"
    "v_mfma_f32_16x16x32_bf16 v[68:71], v[164:167], v[184:187], v[68:71]\n"
    "s_add_u32 m0, s68, s78\n"
    "v_exp_f32 v205, v205\n"
    "v_mfma_f32_16x16x32_bf16 v[72:75], v[168:171], v[176:179], v[72:75]\n"
    "s_add_u32 s78, s78, s77\n"
    "v_exp_f32 v206, v206\n"
    "v_mfma_f32_16x16x32_bf16 v[76:79], v[168:171], v[180:183], v[76:79]\n"
    "s_add_u32 s70, s70, 0x2000\n"
    "v_exp_f32 v207, v207\n"
    "v_mfma_f32_16x16x32_bf16 v[80:83], v[168:171], v[184:187], v[80:83]\n"
    "s_waitcnt lgkmcnt(0)\n"
    "v_exp_f32 v208, v208\n"
    "v_mfma_f32_16x16x32_bf16 v[84:87], v[172:175], v[176:179], v[84:87]\n"
    "v_exp_f32 v209, v209\n"
    "v_mfma_f32_16x16x32_bf16 v[88:91], v[172:175], v[180:183], v[88:91]\n"
    "s_waitcnt vmcnt(0)\n"
    "v_exp_f32 v210, v210\n"
    "v_mfma_f32_16x16x32_bf16 v[92:95], v[172:175], v[184:187], v[92:95]\n"
    "s_barrier\n"
    "v_exp_f32 v211, v211\n"
    "ds_read_b64_tr_b16 v[160:161], v243 offset:8192\n"
    "ds_read_b64_tr_b16 v[162:163], v243 offset:12288\n"
    "ds_read_b64_tr_b16 v[164:165], v244 offset:8192\n"
    "ds_read_b64_tr_b16 v[166:167], v244 offset:12288\n"
    "ds_read_b64_tr_b16 v[168:169], v243 offset:8448\n"
    "ds_read_b64_tr_b16 v[170:171], v243 offset:12544\n"
    "ds_read_b64_tr_b16 v[172:173], v244 offset:8448\n"
    "ds_read_b64_tr_b16 v[174:175], v244 offset:12544\n"
    "v_cvt_pk_bf16_f32 v194, v204, v205\n"
    "v_cvt_pk_bf16_f32 v195, v206, v207\n"
    "v_cvt_pk_bf16_f32 v198, v208, v209\n"
    "v_cvt_pk_bf16_f32 v199, v210, v211\n"
    "ds_read_b64_tr_b16 v[144:145], v243 offset:8704\n"
    "ds_read_b64_tr_b16 v[146:147], v243 offset:12800\n"
    "ds_read_b64_tr_b16 v[148:149], v244 offset:8704\n"
    "ds_read_b64_tr_b16 v[150:151], v244 offset:12800\n"
    "ds_read_b64_tr_b16 v[152:153], v243 offset:8960\n"
    "ds_read_b64_tr_b16 v[154:155], v243 offset:13056\n"
    "ds_read_b64_tr_b16 v[156:157], v244 offset:8960\n"
    "ds_read_b64_tr_b16 v[158:159], v244 offset:13056\n"
    "v_mfma_f32_16x16x32_bf16 v[212:215], v[236:239], v[188:191], 0\n"
    "v_mfma_f32_16x16x32_bf16 v[216:219], v[236:239], v[192:195], 0\n"
    "v_mfma_f32_16x16x32_bf16 v[220:223], v[236:239], v[196:199], 0\n"
    "s_nop 7\n"
    "v_max3_f32 v250, v212, v216, v220\n"
    "v_cmp_lt_f32 vcc, s79, v250\n"
    "s_waitcnt lgkmcnt(0)\n"
    "s_cbranch_vccnz Lrj5%=\n"
    "Lback5%=:\n"
    "v_mfma_f32_16x16x32_bf16 v[0:3], v[160:163], v[188:191], v[0:3]\n"
    "v_add_f32 v240, v212, v240\n"
    "v_mfma_f32_16x16x32_bf16 v[12:15], v[164:167], v[188:191], v[12:15]\n"
    "v_mfma_f32_16x16x32_bf16 v[24:27], v[168:171], v[188:191], v[24:27]\n"
    "v_mfma_f32_16x16x32_bf16 v[36:39], v[172:175], v[188:191], v[36:39]\n"
    "v_mfma_f32_16x16x32_bf16 v[48:51], v[144:147], v[188:191], v[48:51]\n"
    "v_mfma_f32_16x16x32_bf16 v[60:63], v[148:151], v[188:191], v[60:63]\n"
    "v_mfma_f32_16x16x32_bf16 v[72:75], v[152:155], v[188:191], v[72:75]\n"
    "v_mfma_f32_16x16x32_bf16 v[84:87], v[156:159], v[188:191], v[84:87]\n"
    "v_mfma_f32_16x16x32_bf16 v[4:7], v[160:163], v[192:195], v[4:7]\n"
    "v_add_f32 v241, v216, v241\n"
    "v_mfma_f32_16x16x32_bf16 v[16:19], v[164:167], v[192:195], v[16:19]\n"
    "v_mfma_f32_16x16x32_bf16 v[28:31], v[168:171], v[192:195], v[28:31]\n"
    "v_mfma_f32_16x16x32_bf16 v[40:43], v[172:175], v[192:195], v[40:43]\n"
    "v_mfma_f32_16x16x32_bf16 v[52:55], v[144:147], v[192:195], v[52:55]\n"
    "v_mfma_f32_16x16x32_bf16 v[64:67], v[148:151], v[192:195], v[64:67]\n"
    "v_mfma_f32_16x16x32_bf16 v[76:79], v[152:155], v[192:195], v[76:79]\n"
    "v_mfma_f32_16x16x32_bf16 v[88:91], v[156:159], v[192:195], v[88:91]\n"
    "v_mfma_f32_16x16x32_bf16 v[8:11], v[160:163], v[196:199], v[8:11]\n"
    "v_add_f32 v242, v220, v242\n"
    "v_mfma_f32_16x16x32_bf16 v[20:23], v[164:167], v[196:199], v[20:23]\n"
    "v_mfma_f32_16x16x32_bf16 v[32:35], v[168:171], v[196:199], v[32:35]\n"
    "v_mfma_f32_16x16x32_bf16 v[44:47], v[172:175], v[196:199], v[44:47]\n"
    "v_mfma_f32_16x16x32_bf16 v[56:59], v[144:147], v[196:199], v[56:59]\n"
    "v_mfma_f32_16x16x32_bf16 v[68:71], v[148:151], v[196:199], v[68:71]\n"
    "v_mfma_f32_16x16x32_bf16 v[80:83], v[152:155], v[196:199], v[80:83]\n"
    "v_mfma_f32_16x16x32_bf16 v[92:95], v[156:159], v[196:199], v[92:95]\n"
    "s_branch Lepi%=\n"
    "Lrj0%=:\n"
    "s_mov_b32 s9, 0\n"
    "s_branch Lrare0%=\n"
    "Lrj1%=:\n"
    "s_mov_b32 s9, 1\n"
    "s_branch Lrare1%=\n"
    "Lrj2%=:\n"
    "s_mov_b32 s9, 2\n"
    "s_branch Lrare0%=\n"
    "Lrj3%=:\n"
    "s_mov_b32 s9, 3\n"
    "s_branch Lrare1%=\n"
    "Lrj4%=:\n"
    "s_mov_b32 s9, 4\n"
    "s_branch Lrare0%=\n"
    "Lrj5%=:\n"
    "s_mov_b32 s9, 5\n"
    "s_branch Lrare1%=\n"
    "Lrare0%=:\n"
    "s_nop 7\n"
    "s_mov_b32 s4, s100\n"
    "s_mov_b32 s5, s101\n"
    "s_mov_b32 s6, 0x40000\n"
    "s_mov_b32 s7, 0x20000\n"
    "s_add_u32 s8, s76, 4096\n"
    "v_bfe_u32 v254, v255, 4, 4\n"
    "v_lshlrev_b32 v254, 8, v254\n"
    "v_lshrrev_b32 v253, 8, v255\n"
    "v_lshl_add_u32 v254, v253, 4, v254\n"
    "buffer_load_dwordx4 v[188:191], v254, s[4:7], s76 offen offset:0 \n"
    "buffer_load_dwordx4 v[192:195], v254, s[4:7], s76 offen offset:64 \n"
    "buffer_load_dwordx4 v[196:199], v254, s[4:7], s76 offen offset:128 \n"
    "buffer_load_dwordx4 v[212:215], v254, s[4:7], s76 offen offset:192 \n"
    "s_waitcnt vmcnt(0)\n"
    "v_mfma_f32_16x16x32_bf16 v[176:179], v[188:191], v[96:99], v[224:227]\n"
    "v_mfma_f32_16x16x32_bf16 v[176:179], v[192:195], v[108:111], v[176:179]\n"
    "v_mfma_f32_16x16x32_bf16 v[176:179], v[196:199], v[120:123], v[176:179]\n"
    "v_mfma_f32_16x16x32_bf16 v[176:179], v[212:215], v[132:135], v[176:179]\n"
    "v_mfma_f32_16x16x32_bf16 v[180:183], v[188:191], v[100:103], v[228:231]\n"
    "v_mfma_f32_16x16x32_bf16 v[180:183], v[192:195], v[112:115], v[180:183]\n"
    "v_mfma_f32_16x16x32_bf16 v[180:183], v[196:199], v[124:127], v[180:183]\n"
    "v_mfma_f32_16x16x32_bf16 v[180:183], v[212:215], v[136:139], v[180:183]\n"
    "v_mfma_f32_16x16x32_bf16 v[184:187], v[188:191], v[104:107], v[232:235]\n"
    "v_mfma_f32_16x16x32_bf16 v[184:187], v[192:195], v[116:119], v[184:187]\n"
    "v_mfma_f32_16x16x32_bf16 v[184:187], v[196:199], v[128:131], v[184:187]\n"
    "v_mfma_f32_16x16x32_bf16 v[184:187], v[212:215], v[140:143], v[184:187]\n"
    "s_nop 7\n"
    "buffer_load_dwordx4 v[188:191], v254, s[4:7], s8 offen offset:0 \n"
    "buffer_load_dwordx4 v[192:195], v254, s[4:7], s8 offen offset:64 \n"
    "buffer_load_dwordx4 v[196:199], v254, s[4:7], s8 offen offset:128 \n"
    "buffer_load_dwordx4 v[212:215], v254, s[4:7], s8 offen offset:192 \n"
    "s_waitcnt vmcnt(0)\n"
    "v_mfma_f32_16x16x32_bf16 v[200:203], v[188:191], v[96:99], v[224:227]\n"
    "v_mfma_f32_16x16x32_bf16 v[200:203], v[192:195], v[108:111], v[200:203]\n"
    "v_mfma_f32_16x16x32_bf16 v[200:203], v[196:199], v[120:123], v[200:203]\n"
    "v_mfma_f32_16x16x32_bf16 v[200:203], v[212:215], v[132:135], v[200:203]\n"
    "v_mfma_f32_16x16x32_bf16 v[204:207], v[188:191], v[100:103], v[228:231]\n"
    "v_mfma_f32_16x16x32_bf16 v[204:207], v[192:195], v[112:115], v[204:207]\n"
    "v_mfma_f32_16x16x32_bf16 v[204:207], v[196:199], v[124:127], v[204:207]\n"
    "v_mfma_f32_16x16x32_bf16 v[204:207], v[212:215], v[136:139], v[204:207]\n"
    "v_mfma_f32_16x16x32_bf16 v[208:211], v[188:191], v[104:107], v[232:235]\n"
    "v_mfma_f32_16x16x32_bf16 v[208:211], v[192:195], v[116:119], v[208:211]\n"
    "v_mfma_f32_16x16x32_bf16 v[208:211], v[196:199], v[128:131], v[208:211]\n"
    "v_mfma_f32_16x16x32_bf16 v[208:211], v[212:215], v[140:143], v[208:211]\n"
    "s_nop 7\n"
    "v_max3_f32 v250, v176, v177, v178\n"
    "v_max3_f32 v253, v179, v200, v201\n"
    "v_max3_f32 v250, v250, v202, v203\n"
    "v_max_f32 v250, v250, v253\n"
    "v_mov_b32 v253, v250\n"
    "s_nop 1\n"
    "v_permlane16_swap_b32 v250, v253\n"
    "s_nop 0\n"
    "v_max_f32 v250, v250, v253\n"
    "v_mov_b32 v253, v250\n"
    "s_nop 1\n"
    "v_permlane32_swap_b32 v250, v253\n"
    "s_nop 0\n"
    "v_max3_f32 v250, v250, v253, 0\n"
    "v_sub_f32 v176, v176, v250\n"
    "v_sub_f32 v200, v200, v250\n"
    "v_sub_f32 v224, v224, v250\n"
    "v_sub_f32 v177, v177, v250\n"
    "v_sub_f32 v201, v201, v250\n"
    "v_sub_f32 v225, v225, v250\n"
    "v_sub_f32 v178, v178, v250\n"
    "v_sub_f32 v202, v202, v250\n"
    "v_sub_f32 v226, v226, v250\n"
    "v_sub_f32 v179, v179, v250\n"
    "v_sub_f32 v203, v203, v250\n"
    "v_sub_f32 v227, v227, v250\n"
    "v_exp_f32 v176, v176\n"
    "v_exp_f32 v200, v200\n"
    "v_exp_f32 v177, v177\n"
    "v_exp_f32 v201, v201\n"
    "v_exp_f32 v178, v178\n"
    "v_exp_f32 v202, v202\n"
    "v_exp_f32 v179, v179\n"
    "v_exp_f32 v203, v203\n"
    "v_max3_f32 v251, v180, v181, v182\n"
    "v_max3_f32 v253, v183, v204, v205\n"
    "v_max3_f32 v251, v251, v206, v207\n"
    "v_max_f32 v251, v251, v253\n"
    "v_mov_b32 v253, v251\n"
    "s_nop 1\n"
    "v_permlane16_swap_b32 v251, v253\n"
    "s_nop 0\n"
    "v_max_f32 v251, v251, v253\n"
    "v_mov_b32 v253, v251\n"
    "s_nop 1\n"
    "v_permlane32_swap_b32 v251, v253\n"
    "s_nop 0\n"
    "v_max3_f32 v251, v251, v253, 0\n"
    "v_sub_f32 v180, v180, v251\n"
    "v_sub_f32 v204, v204, v251\n"
    "v_sub_f32 v228, v228, v251\n"
    "v_sub_f32 v181, v181, v251\n"
    "v_sub_f32 v205, v205, v251\n"
    "v_sub_f32 v229, v229, v251\n"
    "v_sub_f32 v182, v182, v251\n"
    "v_sub_f32 v206, v206, v251\n"
    "v_sub_f32 v230, v230, v251\n"
    "v_sub_f32 v183, v183, v251\n"
    "v_sub_f32 v207, v207, v251\n"
    "v_sub_f32 v231, v231, v251\n"
    "v_exp_f32 v180, v180\n"
    "v_exp_f32 v204, v204\n"
    "v_exp_f32 v181, v181\n"
    "v_exp_f32 v205, v205\n"
    "v_exp_f32 v182, v182\n"
    "v_exp_f32 v206, v206\n"
    "v_exp_f32 v183, v183\n"
    "v_exp_f32 v207, v207\n"
    "v_max3_f32 v252, v184, v185, v186\n"
    "v_max3_f32 v253, v187, v208, v209\n"
    "v_max3_f32 v252, v252, v210, v211\n"
    "v_max_f32 v252, v252, v253\n"
    "v_mov_b32 v253, v252\n"
    "s_nop 1\n"
    "v_permlane16_swap_b32 v252, v253\n"
    "s_nop 0\n"
    "v_max_f32 v252, v252, v253\n"
    "v_mov_b32 v253, v252\n"
    "s_nop 1\n"
    "v_permlane32_swap_b32 v252, v253\n"
    "s_nop 0\n"
    "v_max3_f32 v252, v252, v253, 0\n"
    "v_sub_f32 v184, v184, v252\n"
    "v_sub_f32 v208, v208, v252\n"
    "v_sub_f32 v232, v232, v252\n"
    "v_sub_f32 v185, v185, v252\n"
    "v_sub_f32 v209, v209, v252\n"
    "v_sub_f32 v233, v233, v252\n"
    "v_sub_f32 v186, v186, v252\n"
    "v_sub_f32 v210, v210, v252\n"
    "v_sub_f32 v234, v234, v252\n"
    "v_sub_f32 v187, v187, v252\n"
    "v_sub_f32 v211, v211, v252\n"
    "v_sub_f32 v235, v235, v252\n"
    "v_exp_f32 v184, v184\n"
    "v_exp_f32 v208, v208\n"
    "v_exp_f32 v185, v185\n"
    "v_exp_f32 v209, v209\n"
    "v_exp_f32 v186, v186\n"
    "v_exp_f32 v210, v210\n"
    "v_exp_f32 v187, v187\n"
    "v_exp_f32 v211, v211\n"
    "v_cvt_pk_bf16_f32 v176, v176, v177\n"
    "v_cvt_pk_bf16_f32 v177, v178, v179\n"
    "v_cvt_pk_bf16_f32 v178, v200, v201\n"
    "v_cvt_pk_bf16_f32 v179, v202, v203\n"
    "v_cvt_pk_bf16_f32 v180, v180, v181\n"
    "v_cvt_pk_bf16_f32 v181, v182, v183\n"
    "v_cvt_pk_bf16_f32 v182, v204, v205\n"
    "v_cvt_pk_bf16_f32 v183, v206, v207\n"
    "v_cvt_pk_bf16_f32 v184, v184, v185\n"
    "v_cvt_pk_bf16_f32 v185, v186, v187\n"
    "v_cvt_pk_bf16_f32 v186, v208, v209\n"
    "v_cvt_pk_bf16_f32 v187, v210, v211\n"
    "v_sub_f32 v250, 0, v250\n"
    "v_exp_f32 v250, v250\n"
    "s_nop 0\n"
    "v_mul_f32 v240, v240, v250\n"
    "v_mul_f32 v0, v0, v250\n"
    "v_mul_f32 v1, v1, v250\n"
    "v_mul_f32 v2, v2, v250\n"
    "v_mul_f32 v3, v3, v250\n"
    "v_mul_f32 v12, v12, v250\n"
    "v_mul_f32 v13, v13, v250\n"
    "v_mul_f32 v14, v14, v250\n"
    "v_mul_f32 v15, v15, v250\n"
    "v_mul_f32 v24, v24, v250\n"
    "v_mul_f32 v25, v25, v250\n"
    "v_mul_f32 v26, v26, v250\n"
    "v_mul_f32 v27, v27, v250\n"
    "v_mul_f32 v36, v36, v250\n"
    "v_mul_f32 v37, v37, v250\n"
    "v_mul_f32 v38, v38, v250\n"
    "v_mul_f32 v39, v39, v250\n"
    "v_mul_f32 v48, v48, v250\n"
    "v_mul_f32 v49, v49, v250\n"
    "v_mul_f32 v50, v50, v250\n"
    "v_mul_f32 v51, v51, v250\n"
    "v_mul_f32 v60, v60, v250\n"
    "v_mul_f32 v61, v61, v250\n"
    "v_mul_f32 v62, v62, v250\n"
    "v_mul_f32 v63, v63, v250\n"
    "v_mul_f32 v72, v72, v250\n"
    "v_mul_f32 v73, v73, v250\n"
    "v_mul_f32 v74, v74, v250\n"
    "v_mul_f32 v75, v75, v250\n"
    "v_mul_f32 v84, v84, v250\n"
    "v_mul_f32 v85, v85, v250\n"
    "v_mul_f32 v86, v86, v250\n"
    "v_mul_f32 v87, v87, v250\n"
    "v_sub_f32 v251, 0, v251\n"
    "v_exp_f32 v251, v251\n"
    "s_nop 0\n"
    "v_mul_f32 v241, v241, v251\n"
    "v_mul_f32 v4, v4, v251\n"
    "v_mul_f32 v5, v5, v251\n"
    "v_mul_f32 v6, v6, v251\n"
    "v_mul_f32 v7, v7, v251\n"
    "v_mul_f32 v16, v16, v251\n"
    "v_mul_f32 v17, v17, v251\n"
    "v_mul_f32 v18, v18, v251\n"
    "v_mul_f32 v19, v19, v251\n"
    "v_mul_f32 v28, v28, v251\n"
    "v_mul_f32 v29, v29, v251\n"
    "v_mul_f32 v30, v30, v251\n"
    "v_mul_f32 v31, v31, v251\n"
    "v_mul_f32 v40, v40, v251\n"
    "v_mul_f32 v41, v41, v251\n"
    "v_mul_f32 v42, v42, v251\n"
    "v_mul_f32 v43, v43, v251\n"
    "v_mul_f32 v52, v52, v251\n"
    "v_mul_f32 v53, v53, v251\n"
    "v_mul_f32 v54, v54, v251\n"
    "v_mul_f32 v55, v55, v251\n"
    "v_mul_f32 v64, v64, v251\n"
    "v_mul_f32 v65, v65, v251\n"
    "v_mul_f32 v66, v66, v251\n"
    "v_mul_f32 v67, v67, v251\n"
    "v_mul_f32 v76, v76, v251\n"
    "v_mul_f32 v77, v77, v251\n"
    "v_mul_f32 v78, v78, v251\n"
    "v_mul_f32 v79, v79, v251\n"
    "v_mul_f32 v88, v88, v251\n"
    "v_mul_f32 v89, v89, v251\n"
    "v_mul_f32 v90, v90, v251\n"
    "v_mul_f32 v91, v91, v251\n"
    "v_sub_f32 v252, 0, v252\n"
    "v_exp_f32 v252, v252\n"
    "s_nop 0\n"
    "v_mul_f32 v242, v242, v252\n"
    "v_mul_f32 v8, v8, v252\n"
    "v_mul_f32 v9, v9, v252\n"
    "v_mul_f32 v10, v10, v252\n"
    "v_mul_f32 v11, v11, v252\n"
    "v_mul_f32 v20, v20, v252\n"
    "v_mul_f32 v21, v21, v252\n"
    "v_mul_f32 v22, v22, v252\n"
    "v_mul_f32 v23, v23, v252\n"
    "v_mul_f32 v32, v32, v252\n"
    "v_mul_f32 v33, v33, v252\n"
    "v_mul_f32 v34, v34, v252\n"
    "v_mul_f32 v35, v35, v252\n"
    "v_mul_f32 v44, v44, v252\n"
    "v_mul_f32 v45, v45, v252\n"
    "v_mul_f32 v46, v46, v252\n"
    "v_mul_f32 v47, v47, v252\n"
    "v_mul_f32 v56, v56, v252\n"
    "v_mul_f32 v57, v57, v252\n"
    "v_mul_f32 v58, v58, v252\n"
    "v_mul_f32 v59, v59, v252\n"
    "v_mul_f32 v68, v68, v252\n"
    "v_mul_f32 v69, v69, v252\n"
    "v_mul_f32 v70, v70, v252\n"
    "v_mul_f32 v71, v71, v252\n"
    "v_mul_f32 v80, v80, v252\n"
    "v_mul_f32 v81, v81, v252\n"
    "v_mul_f32 v82, v82, v252\n"
    "v_mul_f32 v83, v83, v252\n"
    "v_mul_f32 v92, v92, v252\n"
    "v_mul_f32 v93, v93, v252\n"
    "v_mul_f32 v94, v94, v252\n"
    "v_mul_f32 v95, v95, v252\n"
    "v_mfma_f32_16x16x32_bf16 v[188:191], v[144:147], v[96:99], v[224:227]\n"
    "v_mfma_f32_16x16x32_bf16 v[188:191], v[148:151], v[108:111], v[188:191]\n"
    "v_mfma_f32_16x16x32_bf16 v[188:191], v[152:155], v[120:123], v[188:191]\n"
    "v_mfma_f32_16x16x32_bf16 v[188:191], v[156:159], v[132:135], v[188:191]\n"
    "v_mfma_f32_16x16x32_bf16 v[192:195], v[144:147], v[100:103], v[228:231]\n"
    "v_mfma_f32_16x16x32_bf16 v[192:195], v[148:151], v[112:115], v[192:195]\n"
    "v_mfma_f32_16x16x32_bf16 v[192:195], v[152:155], v[124:127], v[192:195]\n"
    "v_mfma_f32_16x16x32_bf16 v[192:195], v[156:159], v[136:139], v[192:195]\n"
    "v_mfma_f32_16x16x32_bf16 v[212:215], v[236:239], v[176:179], 0\n"
    "v_mfma_f32_16x16x32_bf16 v[216:219], v[236:239], v[180:183], 0\n"
    "v_mfma_f32_16x16x32_bf16 v[220:223], v[236:239], v[184:187], 0\n"
    "v_mfma_f32_16x16x32_bf16 v[196:199], v[144:147], v[104:107], v[232:235]\n"
    "v_mfma_f32_16x16x32_bf16 v[196:199], v[148:151], v[116:119], v[196:199]\n"
    "v_mfma_f32_16x16x32_bf16 v[196:199], v[152:155], v[128:131], v[196:199]\n"
    "v_mfma_f32_16x16x32_bf16 v[196:199], v[156:159], v[140:143], v[196:199]\n"
    "v_exp_f32 v188, v188\n"
    "v_exp_f32 v189, v189\n"
    "v_exp_f32 v190, v190\n"
    "v_exp_f32 v191, v191\n"
    "v_exp_f32 v192, v192\n"
    "v_exp_f32 v193, v193\n"
    "s_nop 7\n"
    "s_cmp_eq_u32 s9, 0\n"
    "s_cbranch_scc1 Lback0%=\n"
    "s_cmp_eq_u32 s9, 2\n"
    "s_cbranch_scc1 Lback2%=\n"
    "s_branch Lback4%=\n"
    "Lrare1%=:\n"
    "s_nop 7\n"
    "s_mov_b32 s4, s100\n"
    "s_mov_b32 s5, s101\n"
    "s_mov_b32 s6, 0x40000\n"
    "s_mov_b32 s7, 0x20000\n"
    "s_add_u32 s8, s76, 4096\n"
    "v_bfe_u32 v254, v255, 4, 4\n"
    "v_lshlrev_b32 v254, 8, v254\n"
    "v_lshrrev_b32 v253, 8, v255\n"
    "v_lshl_add_u32 v254, v253, 4, v254\n"
    "buffer_load_dwordx4 v[176:179], v254, s[4:7], s76 offen offset:0 \n"
    "buffer_load_dwordx4 v[180:183], v254, s[4:7], s76 offen offset:64 \n"
    "buffer_load_dwordx4 v[184:187], v254, s[4:7], s76 offen offset:128 \n"
    "buffer_load_dwordx4 v[212:215], v254, s[4:7], s76 offen offset:192 \n"
    "s_waitcnt vmcnt(0)\n"
    "v_mfma_f32_16x16x32_bf16 v[188:191], v[176:179], v[96:99], v[224:227]\n"
    "v_mfma_f32_16x16x32_bf16 v[188:191], v[180:183], v[108:111], v[188:191]\n"
    "v_mfma_f32_16x16x32_bf16 v[188:191], v[184:187], v[120:123], v[188:191]\n"
    "v_mfma_f32_16x16x32_bf16 v[188:191], v[212:215], v[132:135], v[188:191]\n"
    "v_mfma_f32_16x16x32_bf16 v[192:195], v[176:179], v[100:103], v[228:231]\n"
    "v_mfma_f32_16x16x32_bf16 v[192:195], v[180:183], v[112:115], v[192:195]\n"
    "v_mfma_f32_16x16x32_bf16 v[192:195], v[184:187], v[124:127], v[192:195]\n"
    "v_mfma_f32_16x16x32_bf16 v[192:195], v[212:215], v[136:139], v[192:195]\n"
    "v_mfma_f32_16x16x32_bf16 v[196:199], v[176:179], v[104:107], v[232:235]\n"
    "v_mfma_f32_16x16x32_bf16 v[196:199], v[180:183], v[116:119], v[196:199]\n"
    "v_mfma_f32_16x16x32_bf16 v[196:199], v[184:187], v[128:131], v[196:199]\n"
    "v_mfma_f32_16x16x32_bf16 v[196:199], v[212:215], v[140:143], v[196:199]\n"
    "s_nop 7\n"
    "buffer_load_dwordx4 v[176:179], v254, s[4:7], s8 offen offset:0 \n"
    "buffer_load_dwordx4 v[180:183], v254, s[4:7], s8 offen offset:64 \n"
    "buffer_load_dwordx4 v[184:187], v254, s[4:7], s8 offen offset:128 \n"
    "buffer_load_dwordx4 v[212:215], v254, s[4:7], s8 offen offset:192 \n"
    "s_waitcnt vmcnt(0)\n"
    "v_mfma_f32_16x16x32_bf16 v[200:203], v[176:179], v[96:99], v[224:227]\n"
    "v_mfma_f32_16x16x32_bf16 v[200:203], v[180:183], v[108:111], v[200:203]\n"
    "v_mfma_f32_16x16x32_bf16 v[200:203], v[184:187], v[120:123], v[200:203]\n"
    "v_mfma_f32_16x16x32_bf16 v[200:203], v[212:215], v[132:135], v[200:203]\n"
    "v_mfma_f32_16x16x32_bf16 v[204:207], v[176:179], v[100:103], v[228:231]\n"
    "v_mfma_f32_16x16x32_bf16 v[204:207], v[180:183], v[112:115], v[204:207]\n"
    "v_mfma_f32_16x16x32_bf16 v[204:207], v[184:187], v[124:127], v[204:207]\n"
    "v_mfma_f32_16x16x32_bf16 v[204:207], v[212:215], v[136:139], v[204:207]\n"
    "v_mfma_f32_16x16x32_bf16 v[208:211], v[176:179], v[104:107], v[232:235]\n"
    "v_mfma_f32_16x16x32_bf16 v[208:211], v[180:183], v[116:119], v[208:211]\n"
    "v_mfma_f32_16x16x32_bf16 v[208:211], v[184:187], v[128:131], v[208:211]\n"
    "v_mfma_f32_16x16x32_bf16 v[208:211], v[212:215], v[140:143], v[208:211]\n"
    "s_nop 7\n"
    "v_max3_f32 v250, v188, v189, v190\n"
    "v_max3_f32 v253, v191, v200, v201\n"
    "v_max3_f32 v250, v250, v202, v203\n"
    "v_max_f32 v250, v250, v253\n"
    "v_mov_b32 v253, v250\n"
    "s_nop 1\n"
    "v_permlane16_swap_b32 v250, v253\n"
    "s_nop 0\n"
    "v_max_f32 v250, v250, v253\n"
    "v_mov_b32 v253, v250\n"
    "s_nop 1\n"
    "v_permlane32_swap_b32 v250, v253\n"
    "s_nop 0\n"
    "v_max3_f32 v250, v250, v253, 0\n"
    "v_sub_f32 v188, v188, v250\n"
    "v_sub_f32 v200, v200, v250\n"
    "v_sub_f32 v224, v224, v250\n"
    "v_sub_f32 v189, v189, v250\n"
    "v_sub_f32 v201, v201, v250\n"
    "v_sub_f32 v225, v225, v250\n"
    "v_sub_f32 v190, v190, v250\n"
    "v_sub_f32 v202, v202, v250\n"
    "v_sub_f32 v226, v226, v250\n"
    "v_sub_f32 v191, v191, v250\n"
    "v_sub_f32 v203, v203, v250\n"
    "v_sub_f32 v227, v227, v250\n"
    "v_exp_f32 v188, v188\n"
    "v_exp_f32 v200, v200\n"
    "v_exp_f32 v189, v189\n"
    "v_exp_f32 v201, v201\n"
    "v_exp_f32 v190, v190\n"
    "v_exp_f32 v202, v202\n"
    "v_exp_f32 v191, v191\n"
    "v_exp_f32 v203, v203\n"
    "v_max3_f32 v251, v192, v193, v194\n"
    "v_max3_f32 v253, v195, v204, v205\n"
    "v_max3_f32 v251, v251, v206, v207\n"
    "v_max_f32 v251, v251, v253\n"
    "v_mov_b32 v253, v251\n"
    "s_nop 1\n"
    "v_permlane16_swap_b32 v251, v253\n"
    "s_nop 0\n"
    "v_max_f32 v251, v251, v253\n"
    "v_mov_b32 v253, v251\n"
    "s_nop 1\n"
    "v_permlane32_swap_b32 v251, v253\n"
    "s_nop 0\n"
    "v_max3_f32 v251, v251, v253, 0\n"
    "v_sub_f32 v192, v192, v251\n"
    "v_sub_f32 v204, v204, v251\n"
    "v_sub_f32 v228, v228, v251\n"
    "v_sub_f32 v193, v193, v251\n"
    "v_sub_f32 v205, v205, v251\n"
    "v_sub_f32 v229, v229, v251\n"
    "v_sub_f32 v194, v194, v251\n"
    "v_sub_f32 v206, v206, v251\n"
    "v_sub_f32 v230, v230, v251\n"
    "v_sub_f32 v195, v195, v251\n"
    "v_sub_f32 v207, v207, v251\n"
    "v_sub_f32 v231, v231, v251\n"
    "v_exp_f32 v192, v192\n"
    "v_exp_f32 v204, v204\n"
    "v_exp_f32 v193, v193\n"
    "v_exp_f32 v205, v205\n"
    "v_exp_f32 v194, v194\n"
    "v_exp_f32 v206, v206\n"
    "v_exp_f32 v195, v195\n"
    "v_exp_f32 v207, v207\n"
    "v_max3_f32 v252, v196, v197, v198\n"
    "v_max3_f32 v253, v199, v208, v209\n"
    "v_max3_f32 v252, v252, v210, v211\n"
    "v_max_f32 v252, v252, v253\n"
    "v_mov_b32 v253, v252\n"
    "s_nop 1\n"
    "v_permlane16_swap_b32 v252, v253\n"
    "s_nop 0\n"
    "v_max_f32 v252, v252, v253\n"
    "v_mov_b32 v253, v252\n"
    "s_nop 1\n"
    "v_permlane32_swap_b32 v252, v253\n"
    "s_nop 0\n"
    "v_max3_f32 v252, v252, v253, 0\n"
    "v_sub_f32 v196, v196, v252\n"
    "v_sub_f32 v208, v208, v252\n"
    "v_sub_f32 v232, v232, v252\n"
    "v_sub_f32 v197, v197, v252\n"
    "v_sub_f32 v209, v209, v252\n"
    "v_sub_f32 v233, v233, v252\n"
    "v_sub_f32 v198, v198, v252\n"
    "v_sub_f32 v210, v210, v252\n"
    "v_sub_f32 v234, v234, v252\n"
    "v_sub_f32 v199, v199, v252\n"
    "v_sub_f32 v211, v211, v252\n"
    "v_sub_f32 v235, v235, v252\n"
    "v_exp_f32 v196, v196\n"
    "v_exp_f32 v208, v208\n"
    "v_exp_f32 v197, v197\n"
    "v_exp_f32 v209, v209\n"
    "v_exp_f32 v198, v198\n"
    "v_exp_f32 v210, v210\n"
    "v_exp_f32 v199, v199\n"
    "v_exp_f32 v211, v211\n"
    "v_cvt_pk_bf16_f32 v188, v188, v189\n"
    "v_cvt_pk_bf16_f32 v189, v190, v191\n"
    "v_cvt_pk_bf16_f32 v190, v200, v201\n"
    "v_cvt_pk_bf16_f32 v191, v202, v203\n"
    "v_cvt_pk_bf16_f32 v192, v192, v193\n"
    "v_cvt_pk_bf16_f32 v193, v194, v195\n"
    "v_cvt_pk_bf16_f32 v194, v204, v205\n"
    "v_cvt_pk_bf16_f32 v195, v206, v207\n"
    "v_cvt_pk_bf16_f32 v196, v196, v197\n"
    "v_cvt_pk_bf16_f32 v197, v198, v199\n"
    "v_cvt_pk_bf16_f32 v198, v208, v209\n"
    "v_cvt_pk_bf16_f32 v199, v210, v211\n"
    "v_sub_f32 v250, 0, v250\n"
    "v_exp_f32 v250, v250\n"
    "s_nop 0\n"
    "v_mul_f32 v240, v240, v250\n"
    "v_mul_f32 v0, v0, v250\n"
    "v_mul_f32 v1, v1, v250\n"
    "v_mul_f32 v2, v2, v250\n"
    "v_mul_f32 v3, v3, v250\n"
    "v_mul_f32 v12, v12, v250\n"
    "v_mul_f32 v13, v13, v250\n"
    "v_mul_f32 v14, v14, v250\n"
    "v_mul_f32 v15, v15, v250\n"
    "v_mul_f32 v24, v24, v250\n"
    "v_mul_f32 v25, v25, v250\n"
    "v_mul_f32 v26, v26, v250\n"
    "v_mul_f32 v27, v27, v250\n"
    "v_mul_f32 v36, v36, v250\n"
    "v_mul_f32 v37, v37, v250\n"
    "v_mul_f32 v38, v38, v250\n"
    "v_mul_f32 v39, v39, v250\n"
    "v_mul_f32 v48, v48, v250\n"
    "v_mul_f32 v49, v49, v250\n"
    "v_mul_f32 v50, v50, v250\n"
    "v_mul_f32 v51, v51, v250\n"
    "v_mul_f32 v60, v60, v250\n"
    "v_mul_f32 v61, v61, v250\n"
    "v_mul_f32 v62, v62, v250\n"
    "v_mul_f32 v63, v63, v250\n"
    "v_mul_f32 v72, v72, v250\n"
    "v_mul_f32 v73, v73, v250\n"
    "v_mul_f32 v74, v74, v250\n"
    "v_mul_f32 v75, v75, v250\n"
    "v_mul_f32 v84, v84, v250\n"
    "v_mul_f32 v85, v85, v250\n"
    "v_mul_f32 v86, v86, v250\n"
    "v_mul_f32 v87, v87, v250\n"
    "v_sub_f32 v251, 0, v251\n"
    "v_exp_f32 v251, v251\n"
    "s_nop 0\n"
    "v_mul_f32 v241, v241, v251\n"
    "v_mul_f32 v4, v4, v251\n"
    "v_mul_f32 v5, v5, v251\n"
    "v_mul_f32 v6, v6, v251\n"
    "v_mul_f32 v7, v7, v251\n"
    "v_mul_f32 v16, v16, v251\n"
    "v_mul_f32 v17, v17, v251\n"
    "v_mul_f32 v18, v18, v251\n"
    "v_mul_f32 v19, v19, v251\n"
    "v_mul_f32 v28, v28, v251\n"
    "v_mul_f32 v29, v29, v251\n"
    "v_mul_f32 v30, v30, v251\n"
    "v_mul_f32 v31, v31, v251\n"
    "v_mul_f32 v40, v40, v251\n"
    "v_mul_f32 v41, v41, v251\n"
    "v_mul_f32 v42, v42, v251\n"
    "v_mul_f32 v43, v43, v251\n"
    "v_mul_f32 v52, v52, v251\n"
    "v_mul_f32 v53, v53, v251\n"
    "v_mul_f32 v54, v54, v251\n"
    "v_mul_f32 v55, v55, v251\n"
    "v_mul_f32 v64, v64, v251\n"
    "v_mul_f32 v65, v65, v251\n"
    "v_mul_f32 v66, v66, v251\n"
    "v_mul_f32 v67, v67, v251\n"
    "v_mul_f32 v76, v76, v251\n"
    "v_mul_f32 v77, v77, v251\n"
    "v_mul_f32 v78, v78, v251\n"
    "v_mul_f32 v79, v79, v251\n"
    "v_mul_f32 v88, v88, v251\n"
    "v_mul_f32 v89, v89, v251\n"
    "v_mul_f32 v90, v90, v251\n"
    "v_mul_f32 v91, v91, v251\n"
    "v_sub_f32 v252, 0, v252\n"
    "v_exp_f32 v252, v252\n"
    "s_nop 0\n"
    "v_mul_f32 v242, v242, v252\n"
    "v_mul_f32 v8, v8, v252\n"
    "v_mul_f32 v9, v9, v252\n"
    "v_mul_f32 v10, v10, v252\n"
    "v_mul_f32 v11, v11, v252\n"
    "v_mul_f32 v20, v20, v252\n"
    "v_mul_f32 v21, v21, v252\n"
    "v_mul_f32 v22, v22, v252\n"
    "v_mul_f32 v23, v23, v252\n"
    "v_mul_f32 v32, v32, v252\n"
    "v_mul_f32 v33, v33, v252\n"
    "v_mul_f32 v34, v34, v252\n"
    "v_mul_f32 v35, v35, v252\n"
    "v_mul_f32 v44, v44, v252\n"
    "v_mul_f32 v45, v45, v252\n"
    "v_mul_f32 v46, v46, v252\n"
    "v_mul_f32 v47, v47, v252\n"
    "v_mul_f32 v56, v56, v252\n"
    "v_mul_f32 v57, v57, v252\n"
    "v_mul_f32 v58, v58, v252\n"
    "v_mul_f32 v59, v59, v252\n"
    "v_mul_f32 v68, v68, v252\n"
    "v_mul_f32 v69, v69, v252\n"
    "v_mul_f32 v70, v70, v252\n"
    "v_mul_f32 v71, v71, v252\n"
    "v_mul_f32 v80, v80, v252\n"
    "v_mul_f32 v81, v81, v252\n"
    "v_mul_f32 v82, v82, v252\n"
    "v_mul_f32 v83, v83, v252\n"
    "v_mul_f32 v92, v92, v252\n"
    "v_mul_f32 v93, v93, v252\n"
    "v_mul_f32 v94, v94, v252\n"
    "v_mul_f32 v95, v95, v252\n"
    "v_mfma_f32_16x16x32_bf16 v[176:179], v[144:147], v[96:99], v[224:227]\n"
    "v_mfma_f32_16x16x32_bf16 v[176:179], v[148:151], v[108:111], v[176:179]\n"
    "v_mfma_f32_16x16x32_bf16 v[176:179], v[152:155], v[120:123], v[176:179]\n"
    "v_mfma_f32_16x16x32_bf16 v[176:179], v[156:159], v[132:135], v[176:179]\n"
    "v_mfma_f32_16x16x32_bf16 v[180:183], v[144:147], v[100:103], v[228:231]\n"
    "v_mfma_f32_16x16x32_bf16 v[180:183], v[148:151], v[112:115], v[180:183]\n"
    "v_mfma_f32_16x16x32_bf16 v[180:183], v[152:155], v[124:127], v[180:183]\n"
    "v_mfma_f32_16x16x32_bf16 v[180:183], v[156:159], v[136:139], v[180:183]\n"
    "v_mfma_f32_16x16x32_bf16 v[212:215], v[236:239], v[188:191], 0\n"
    "v_mfma_f32_16x16x32_bf16 v[216:219], v[236:239], v[192:195], 0\n"
    "v_mfma_f32_16x16x32_bf16 v[220:223], v[236:239], v[196:199], 0\n"
    "v_mfma_f32_16x16x32_bf16 v[184:187], v[144:147], v[104:107], v[232:235]\n"
    "v_mfma_f32_16x16x32_bf16 v[184:187], v[148:151], v[116:119], v[184:187]\n"
    "v_mfma_f32_16x16x32_bf16 v[184:187], v[152:155], v[128:131], v[184:187]\n"
    "v_mfma_f32_16x16x32_bf16 v[184:187], v[156:159], v[140:143], v[184:187]\n"
    "v_exp_f32 v176, v176\n"
    "v_exp_f32 v177, v177\n"
    "v_exp_f32 v178, v178\n"
    "v_exp_f32 v179, v179\n"
    "v_exp_f32 v180, v180\n"
    "v_exp_f32 v181, v181\n"
    "s_nop 7\n"
    "s_cmp_eq_u32 s9, 1\n"
    "s_cbranch_scc1 Lback1%=\n"
    "s_cmp_eq_u32 s9, 3\n"
    "s_cbranch_scc1 Lback3%=\n"
    "s_branch Lback5%=\n"
    "Lepi%=:\n"
    "s_waitcnt lgkmcnt(0)\n"
    "s_nop 7\n"
    "v_rcp_f32 v212, v240\n"
    "v_rcp_f32 v213, v241\n"
    "v_rcp_f32 v214, v242\n"
    "v_mul_f32 v0, v0, v212\n"
    "v_mul_f32 v1, v1, v212\n"
    "v_mul_f32 v2, v2, v212\n"
    "v_mul_f32 v3, v3, v212\n"
    "v_mul_f32 v12, v12, v212\n"
    "v_mul_f32 v13, v13, v212\n"
    "v_mul_f32 v14, v14, v212\n"
    "v_mul_f32 v15, v15, v212\n"
    "v_cvt_pk_bf16_f32 v96, v0, v1\n"
    "v_cvt_pk_bf16_f32 v97, v2, v3\n"
    "v_cvt_pk_bf16_f32 v98, v12, v13\n"
    "v_cvt_pk_bf16_f32 v99, v14, v15\n"
    "s_mov_b32 s83, 0\n"
    "buffer_store_dwordx4 v[96:99], v255, s[48:51], s83 offen offset:0 sc1\n"
    "v_mul_f32 v24, v24, v212\n"
    "v_mul_f32 v25, v25, v212\n"
    "v_mul_f32 v26, v26, v212\n"
    "v_mul_f32 v27, v27, v212\n"
    "v_mul_f32 v36, v36, v212\n"
    "v_mul_f32 v37, v37, v212\n"
    "v_mul_f32 v38, v38, v212\n"
    "v_mul_f32 v39, v39, v212\n"
    "v_cvt_pk_bf16_f32 v100, v24, v25\n"
    "v_cvt_pk_bf16_f32 v101, v26, v27\n"
    "v_cvt_pk_bf16_f32 v102, v36, v37\n"
    "v_cvt_pk_bf16_f32 v103, v38, v39\n"
    "s_mov_b32 s83, 12288\n"
    "buffer_store_dwordx4 v[100:103], v255, s[48:51], s83 offen offset:0 sc1\n"
    "v_mul_f32 v48, v48, v212\n"
    "v_mul_f32 v49, v49, v212\n"
    "v_mul_f32 v50, v50, v212\n"
    "v_mul_f32 v51, v51, v212\n"
    "v_mul_f32 v60, v60, v212\n"
    "v_mul_f32 v61, v61, v212\n"
    "v_mul_f32 v62, v62, v212\n"
    "v_mul_f32 v63, v63, v212\n"
    "v_cvt_pk_bf16_f32 v104, v48, v49\n"
    "v_cvt_pk_bf16_f32 v105, v50, v51\n"
    "v_cvt_pk_bf16_f32 v106, v60, v61\n"
    "v_cvt_pk_bf16_f32 v107, v62, v63\n"
    "s_mov_b32 s83, 24576\n"
    "buffer_store_dwordx4 v[104:107], v255, s[48:51], s83 offen offset:0 sc1\n"
    "v_mul_f32 v72, v72, v212\n"
    "v_mul_f32 v73, v73, v212\n"
    "v_mul_f32 v74, v74, v212\n"
    "v_mul_f32 v75, v75, v212\n"
    "v_mul_f32 v84, v84, v212\n"
    "v_mul_f32 v85, v85, v212\n"
    "v_mul_f32 v86, v86, v212\n"
    "v_mul_f32 v87, v87, v212\n"
    "v_cvt_pk_bf16_f32 v108, v72, v73\n"
    "v_cvt_pk_bf16_f32 v109, v74, v75\n"
    "v_cvt_pk_bf16_f32 v110, v84, v85\n"
    "v_cvt_pk_bf16_f32 v111, v86, v87\n"
    "s_mov_b32 s83, 36864\n"
    "buffer_store_dwordx4 v[108:111], v255, s[48:51], s83 offen offset:0 sc1\n"
    "v_mul_f32 v4, v4, v213\n"
    "v_mul_f32 v5, v5, v213\n"
    "v_mul_f32 v6, v6, v213\n"
    "v_mul_f32 v7, v7, v213\n"
    "v_mul_f32 v16, v16, v213\n"
    "v_mul_f32 v17, v17, v213\n"
    "v_mul_f32 v18, v18, v213\n"
    "v_mul_f32 v19, v19, v213\n"
    "v_cvt_pk_bf16_f32 v112, v4, v5\n"
    "v_cvt_pk_bf16_f32 v113, v6, v7\n"
    "v_cvt_pk_bf16_f32 v114, v16, v17\n"
    "v_cvt_pk_bf16_f32 v115, v18, v19\n"
    "s_mov_b32 s83, 0\n"
    "buffer_store_dwordx4 v[112:115], v255, s[48:51], s83 offen offset:1024 sc1\n"
    "v_mul_f32 v28, v28, v213\n"
    "v_mul_f32 v29, v29, v213\n"
    "v_mul_f32 v30, v30, v213\n"
    "v_mul_f32 v31, v31, v213\n"
    "v_mul_f32 v40, v40, v213\n"
    "v_mul_f32 v41, v41, v213\n"
    "v_mul_f32 v42, v42, v213\n"
    "v_mul_f32 v43, v43, v213\n"
    "v_cvt_pk_bf16_f32 v116, v28, v29\n"
    "v_cvt_pk_bf16_f32 v117, v30, v31\n"
    "v_cvt_pk_bf16_f32 v118, v40, v41\n"
    "v_cvt_pk_bf16_f32 v119, v42, v43\n"
    "s_mov_b32 s83, 12288\n"
    "buffer_store_dwordx4 v[116:119], v255, s[48:51], s83 offen offset:1024 sc1\n"
    "v_mul_f32 v52, v52, v213\n"
    "v_mul_f32 v53, v53, v213\n"
    "v_mul_f32 v54, v54, v213\n"
    "v_mul_f32 v55, v55, v213\n"
    "v_mul_f32 v64, v64, v213\n"
    "v_mul_f32 v65, v65, v213\n"
    "v_mul_f32 v66, v66, v213\n"
    "v_mul_f32 v67, v67, v213\n"
    "v_cvt_pk_bf16_f32 v120, v52, v53\n"
    "v_cvt_pk_bf16_f32 v121, v54, v55\n"
    "v_cvt_pk_bf16_f32 v122, v64, v65\n"
    "v_cvt_pk_bf16_f32 v123, v66, v67\n"
    "s_mov_b32 s83, 24576\n"
    "buffer_store_dwordx4 v[120:123], v255, s[48:51], s83 offen offset:1024 sc1\n"
    "v_mul_f32 v76, v76, v213\n"
    "v_mul_f32 v77, v77, v213\n"
    "v_mul_f32 v78, v78, v213\n"
    "v_mul_f32 v79, v79, v213\n"
    "v_mul_f32 v88, v88, v213\n"
    "v_mul_f32 v89, v89, v213\n"
    "v_mul_f32 v90, v90, v213\n"
    "v_mul_f32 v91, v91, v213\n"
    "v_cvt_pk_bf16_f32 v124, v76, v77\n"
    "v_cvt_pk_bf16_f32 v125, v78, v79\n"
    "v_cvt_pk_bf16_f32 v126, v88, v89\n"
    "v_cvt_pk_bf16_f32 v127, v90, v91\n"
    "s_mov_b32 s83, 36864\n"
    "buffer_store_dwordx4 v[124:127], v255, s[48:51], s83 offen offset:1024 sc1\n"
    "v_mul_f32 v8, v8, v214\n"
    "v_mul_f32 v9, v9, v214\n"
    "v_mul_f32 v10, v10, v214\n"
    "v_mul_f32 v11, v11, v214\n"
    "v_mul_f32 v20, v20, v214\n"
    "v_mul_f32 v21, v21, v214\n"
    "v_mul_f32 v22, v22, v214\n"
    "v_mul_f32 v23, v23, v214\n"
    "v_cvt_pk_bf16_f32 v128, v8, v9\n"
    "v_cvt_pk_bf16_f32 v129, v10, v11\n"
    "v_cvt_pk_bf16_f32 v130, v20, v21\n"
    "v_cvt_pk_bf16_f32 v131, v22, v23\n"
    "s_mov_b32 s83, 0\n"
    "buffer_store_dwordx4 v[128:131], v255, s[48:51], s83 offen offset:2048 sc1\n"
    "v_mul_f32 v32, v32, v214\n"
    "v_mul_f32 v33, v33, v214\n"
    "v_mul_f32 v34, v34, v214\n"
    "v_mul_f32 v35, v35, v214\n"
    "v_mul_f32 v44, v44, v214\n"
    "v_mul_f32 v45, v45, v214\n"
    "v_mul_f32 v46, v46, v214\n"
    "v_mul_f32 v47, v47, v214\n"
    "v_cvt_pk_bf16_f32 v132, v32, v33\n"
    "v_cvt_pk_bf16_f32 v133, v34, v35\n"
    "v_cvt_pk_bf16_f32 v134, v44, v45\n"
    "v_cvt_pk_bf16_f32 v135, v46, v47\n"
    "s_mov_b32 s83, 12288\n"
    "buffer_store_dwordx4 v[132:135], v255, s[48:51], s83 offen offset:2048 sc1\n"
    "v_mul_f32 v56, v56, v214\n"
    "v_mul_f32 v57, v57, v214\n"
    "v_mul_f32 v58, v58, v214\n"
    "v_mul_f32 v59, v59, v214\n"
    "v_mul_f32 v68, v68, v214\n"
    "v_mul_f32 v69, v69, v214\n"
    "v_mul_f32 v70, v70, v214\n"
    "v_mul_f32 v71, v71, v214\n"
    "v_cvt_pk_bf16_f32 v136, v56, v57\n"
    "v_cvt_pk_bf16_f32 v137, v58, v59\n"
    "v_cvt_pk_bf16_f32 v138, v68, v69\n"
    "v_cvt_pk_bf16_f32 v139, v70, v71\n"
    "s_mov_b32 s83, 24576\n"
    "buffer_store_dwordx4 v[136:139], v255, s[48:51], s83 offen offset:2048 sc1\n"
    "v_mul_f32 v80, v80, v214\n"
    "v_mul_f32 v81, v81, v214\n"
    "v_mul_f32 v82, v82, v214\n"
    "v_mul_f32 v83, v83, v214\n"
    "v_mul_f32 v92, v92, v214\n"
    "v_mul_f32 v93, v93, v214\n"
    "v_mul_f32 v94, v94, v214\n"
    "v_mul_f32 v95, v95, v214\n"
    "v_cvt_pk_bf16_f32 v140, v80, v81\n"
    "v_cvt_pk_bf16_f32 v141, v82, v83\n"
    "v_cvt_pk_bf16_f32 v142, v92, v93\n"
    "v_cvt_pk_bf16_f32 v143, v94, v95\n"
    "s_mov_b32 s83, 36864\n"
    "buffer_store_dwordx4 v[140:143], v255, s[48:51], s83 offen offset:2048 sc1\n"
    "s_cmp_eq_u32 s72, 0\n"
    "s_cbranch_scc1 Loproj%=\n"
    "s_waitcnt vmcnt(0)\n"
    "s_barrier\n"
    "v_mov_b32 v200, s88\n"
    "v_mov_b32 v201, s89\n"
    "s_mov_b64 exec, 1\n"
    "buffer_store_dwordx2 v[200:201], v255, s[64:67], s73 offen sc1\n"
    "s_mov_b64 exec, -1\n"
    "s_cmp_eq_u32 s72, 1\n"
    "s_cbranch_scc1 Lend%=\n"
    "s_mov_b32 s83, 0x40000\n"
    "Lkeep%=:\n"
    "buffer_load_dwordx4 v[4:7], v255, s[64:67], 0 offen offset:0 sc1\n"
    "buffer_load_dwordx4 v[8:11], v255, s[64:67], 0 offen offset:1024 sc1\n"
    "buffer_load_dwordx4 v[12:15], v255, s[64:67], 0 offen offset:2048 sc1\n"
    "buffer_load_dwordx4 v[16:19], v255, s[64:67], 0 offen offset:3072 sc1\n"
    "s_waitcnt vmcnt(0)\n"
    "s_mov_b64 s[34:35], 0\n"
    "v_cmp_ne_u64 vcc, s[88:89], v[4:5]\n"
    "s_or_b64 s[34:35], s[34:35], vcc\n"
    "v_cmp_ne_u64 vcc, s[88:89], v[6:7]\n"
    "s_or_b64 s[34:35], s[34:35], vcc\n"
    "v_cmp_ne_u64 vcc, s[88:89], v[8:9]\n"
    "s_or_b64 s[34:35], s[34:35], vcc\n"
    "v_cmp_ne_u64 vcc, s[88:89], v[10:11]\n"
    "s_or_b64 s[34:35], s[34:35], vcc\n"
    "v_cmp_ne_u64 vcc, s[88:89], v[12:13]\n"
    "s_or_b64 s[34:35], s[34:35], vcc\n"
    "v_cmp_ne_u64 vcc, s[88:89], v[14:15]\n"
    "s_or_b64 s[34:35], s[34:35], vcc\n"
    "v_cmp_ne_u64 vcc, s[88:89], v[16:17]\n"
    "s_or_b64 s[34:35], s[34:35], vcc\n"
    "v_cmp_ne_u64 vcc, s[88:89], v[18:19]\n"
    "s_or_b64 s[34:35], s[34:35], vcc\n"
    "s_cmp_eq_u64 s[34:35], 0\n"
    "s_cbranch_scc1 Lkeep2%=\n"
    "s_sleep 16\n"
    "s_sub_u32 s83, s83, 1\n"
    "s_cmp_lg_u32 s83, 0\n"
    "s_cbranch_scc1 Lkeep%=\n"
    "Lkeep2%=:\n"
    "s_mov_b64 exec, 1\n"
    "s_movk_i32 s83, 0x1000\n"
    "buffer_store_dwordx2 v[200:201], v255, s[64:67], s83 offen sc1\n"
    "s_mov_b64 exec, -1\n"
    "s_branch Lend%=\n"
    "Loproj%=:\n"
    "s_mul_i32 s19, s68, 5\n"
    "s_sub_u32 s19, s19, s75\n"
    "s_lshr_b32 s19, s19, 2\n"
    "s_sub_u32 s20, s68, s19\n"
    "s_lshr_b32 s83, s20, 2\n"
    "s_mul_i32 s17, s83, 3\n"
    "s_add_u32 s17, s17, s19\n"
    "s_lshr_b32 s18, s20, 1\n"
    "s_add_u32 s18, s18, s19\n"
    "s_add_u32 s18, s18, 12288\n"
    "s_bfe_u32 s21, s71, 0x20010\n"
    "s_bfe_u32 s83, s71, 0x60014\n"
    "s_lshl_b32 s69, s83, 7\n"
    "s_add_u32 s69, s69, 0x3000\n"
    "s_mul_i32 s83, s83, 393216\n"
    "s_add_u32 s83, s83, 0x400000\n"
    "s_add_u32 s44, s64, s83\n"
    "s_addc_u32 s45, s65, 0\n"
    "s_mov_b32 s46, 393216\n"
    "v_lshrrev_b32 v0, 8, v255\n"
    "v_bfe_u32 v1, v255, 6, 2\n"
    "v_bfe_u32 v2, v255, 4, 2\n"
    "v_and_b32 v3, 1, v0\n"
    "v_lshlrev_b32 v4, 10, v1\n"
    "v_lshl_add_u32 v4, v0, 6, v4\n"
    "v_lshl_add_u32 v4, v2, 4, v4\n"
    "s_bfe_u32 s33, s20, 0x1000c\n"
    "s_lshl_b32 s33, s33, 14\n"
    "s_lshr_b32 s34, s20, 13\n"
    "s_lshl_b32 s34, s34, 12\n"
    "s_add_u32 s35, s33, s34\n"
    "v_add_u32 v250, s35, v4\n"
    "s_sub_u32 s35, s33, s34\n"
    "s_add_u32 s35, s35, 11264\n"
    "v_xor_b32 v5, 16, v4\n"
    "v_add_u32 v251, s35, v5\n"
    "v_lshlrev_b32 v5, 10, v0\n"
    "v_lshl_add_u32 v5, v1, 6, v5\n"
    "v_xor_b32 v6, v2, v3\n"
    "v_lshl_add_u32 v5, v6, 4, v5\n"
    "v_and_b32 v6, 1, v2\n"
    "v_lshl_add_u32 v5, v6, 3, v5\n"
    "s_lshr_b32 s33, s33, 5\n"
    "s_add_u32 s33, s33, s19\n"
    "s_add_u32 s33, s33, 12288\n"
    "v_add_u32 v180, s33, v5\n"
    "v_xor_b32 v181, 8, v180\n"
    "v_add_u32 v182, 40960, v180\n"
    "v_add_u32 v183, 40960, v181\n"
    "s_lshr_b32 s34, s20, 13\n"
    "s_mul_i32 s34, s34, 6144\n"
    "s_add_u32 s34, s34, s19\n"
    "v_add_u32 v178, s34, v255\n"
    "v_add_u32 v179, 40960, v178\n"
    "v_bfe_u32 v254, v255, 4, 3\n"
    "v_lshlrev_b32 v254, 3, v254\n"
    "v_mov_b64 v[0:1], 0\n"
    "v_mov_b64 v[2:3], 0\n"
    "v_mov_b64 v[4:5], 0\n"
    "v_mov_b64 v[6:7], 0\n"
    "v_mov_b64 v[8:9], 0\n"
    "v_mov_b64 v[10:11], 0\n"
    "v_mov_b64 v[12:13], 0\n"
    "v_mov_b64 v[14:15], 0\n"
    "v_mov_b64 v[16:17], 0\n"
    "v_mov_b64 v[18:19], 0\n"
    "v_mov_b64 v[20:21], 0\n"
    "v_mov_b64 v[22:23], 0\n"
    "v_mov_b64 v[24:25], 0\n"
    "v_mov_b64 v[26:27], 0\n"
    "v_mov_b64 v[28:29], 0\n"
    "v_mov_b64 v[30:31], 0\n"
    "v_mov_b64 v[32:33], 0\n"
    "v_mov_b64 v[34:35], 0\n"
    "v_mov_b64 v[36:37], 0\n"
    "v_mov_b64 v[38:39], 0\n"
    "v_mov_b64 v[40:41], 0\n"
    "v_mov_b64 v[42:43], 0\n"
    "v_mov_b64 v[44:45], 0\n"
    "v_mov_b64 v[46:47], 0\n"
    "v_mov_b64 v[48:49], 0\n"
    "v_mov_b64 v[50:51], 0\n"
    "v_mov_b64 v[52:53], 0\n"
    "v_mov_b64 v[54:55], 0\n"
    "v_mov_b64 v[56:57], 0\n"
    "v_mov_b64 v[58:59], 0\n"
    "v_mov_b64 v[60:61], 0\n"
    "v_mov_b64 v[62:63], 0\n"
    "v_mov_b64 v[64:65], 0\n"
    "v_mov_b64 v[66:67], 0\n"
    "v_mov_b64 v[68:69], 0\n"
    "v_mov_b64 v[70:71], 0\n"
    "v_mov_b64 v[72:73], 0\n"
    "v_mov_b64 v[74:75], 0\n"
    "v_mov_b64 v[76:77], 0\n"
    "v_mov_b64 v[78:79], 0\n"
    "v_mov_b64 v[80:81], 0\n"
    "v_mov_b64 v[82:83], 0\n"
    "v_mov_b64 v[84:85], 0\n"
    "v_mov_b64 v[86:87], 0\n"
    "v_mov_b64 v[88:89], 0\n"
    "v_mov_b64 v[90:91], 0\n"
    "v_mov_b64 v[92:93], 0\n"
    "v_mov_b64 v[94:95], 0\n"
    "s_waitcnt vmcnt(0)\n"
    "s_barrier\n"
    "v_mov_b32 v200, s88\n"
    "v_mov_b32 v201, s89\n"
    "s_mov_b64 exec, 1\n"
    "buffer_store_dwordx2 v[200:201], v255, s[64:67], s73 offen sc1\n"
    "s_mov_b64 exec, -1\n"
    "s_setprio 0\n"
    "s_cmp_ge_u32 s73, 0x10000000\n"
    "s_cbranch_scc1 Lpg2%=\n"
    "s_add_u32 s83, s73, 8224\n"
    "s_mov_b32 s12, 0x100\n"
    "s_mov_b64 exec, 1\n"
    "Lpg%=:\n"
    "s_sleep 4\n"
    "buffer_load_dwordx2 v[186:187], v255, s[64:67], s83 offen sc1\n"
    "s_waitcnt vmcnt(0)\n"
    "v_cmp_eq_u64 vcc, s[88:89], v[186:187]\n"
    "s_cbranch_vccnz Lpg1%=\n"
    "s_sub_u32 s12, s12, 1\n"
    "s_cmp_lg_u32 s12, 0\n"
    "s_cbranch_scc1 Lpg%=\n"
    "Lpg1%=:\n"
    "s_mov_b64 exec, -1\n"
    "Lpg2%=:\n"
    "s_barrier\n"
    "s_mov_b32 s22, s54\n"
    "s_mov_b32 s15, 0\n"
    "s_lshr_b32 s16, s71, 8\n"
    "s_add_u32 s16, s16, s15\n"
    "s_and_b32 s16, s16, 3\n"
    "s_and_b32 s83, s15, 12\n"
    "s_add_u32 s16, s16, s83\n"
    "s_lshl_b32 s5, s16, 17\n"
    "s_mul_i32 s4, s16, 49152\n"
    "buffer_load_dwordx2 v[176:177], v254, s[64:67], s74 offen sc1\n"
    "s_add_u32 m0, s18, 0\n"
    "s_nop 0\n"
    "buffer_load_dwordx4 v250, s[56:59], s5 offen offset:0  lds\n"
    "buffer_load_dwordx4 v251, s[56:59], s5 offen offset:1024  lds\n"
    "s_add_u32 m0, s17, 0\n"
    "s_add_u32 s5, s5, 0x8000\n"
    "s_add_u32 m0, s18, 20480\n"
    "s_nop 0\n"
    "buffer_load_dwordx4 v250, s[56:59], s5 offen offset:0  lds\n"
    "buffer_load_dwordx4 v251, s[56:59], s5 offen offset:1024  lds\n"
    "s_add_u32 m0, s17, 20480\n"
    "s_add_u32 s5, s5, 0x8000\n"
    "s_add_u32 m0, s18, 40960\n"
    "s_nop 0\n"
    "buffer_load_dwordx4 v250, s[56:59], s5 offen offset:0  lds\n"
    "buffer_load_dwordx4 v251, s[56:59], s5 offen offset:1024  lds\n"
    "s_add_u32 m0, s17, 40960\n"
    "s_add_u32 s5, s5, 0x8000\n"
    "s_add_u32 m0, s18, 61440\n"
    "s_nop 0\n"
    "buffer_load_dwordx4 v250, s[56:59], s5 offen offset:0  lds\n"
    "buffer_load_dwordx4 v251, s[56:59], s5 offen offset:1024  lds\n"
    "s_add_u32 m0, s17, 61440\n"
    "s_add_u32 s5, s5, 0x8000\n"
    "s_waitcnt vmcnt(8)\n"
    "Lprime%=:\n"
    "s_mov_b32 s12, 0x40000\n"
    "Lg0%=:\n"
    "v_cmp_eq_u64 vcc, s[88:89], v[176:177]\n"
    "s_bitcmp1_b32 vcc_lo, s16\n"
    "s_cbranch_scc1 Lgo0%=\n"
    "s_sleep 2\n"
    "buffer_load_dwordx2 v[176:177], v254, s[64:67], s74 offen sc1\n"
    "s_waitcnt vmcnt(0)\n"
    "s_sub_u32 s12, s12, 1\n"
    "s_cmp_lg_u32 s12, 0\n"
    "s_cbranch_scc1 Lg0%=\n"
    "Lgo0%=:\n"
    "s_mov_b32 s54, s22\n"
    "s_mul_i32 s4, s16, 49152\n"
    "s_add_u32 m0, s17, 0\n"
    "s_nop 0\n"
    "buffer_load_dwordx4 v255, s[52:55], s4 offen offset:0 sc1 lds\n"
    "buffer_load_dwordx4 v255, s[52:55], s4 offen offset:1024 sc1 lds\n"
    "buffer_load_dwordx4 v255, s[52:55], s4 offen offset:2048 sc1 lds\n"
    "s_add_u32 s4, s4, 12288\n"
    "s_add_u32 m0, s17, 20480\n"
    "s_nop 0\n"
    "buffer_load_dwordx4 v255, s[52:55], s4 offen offset:0 sc1 lds\n"
    "buffer_load_dwordx4 v255, s[52:55], s4 offen offset:1024 sc1 lds\n"
    "buffer_load_dwordx4 v255, s[52:55], s4 offen offset:2048 sc1 lds\n"
    "s_add_u32 s4, s4, 12288\n"
    "s_add_u32 m0, s17, 40960\n"
    "s_nop 0\n"
    "buffer_load_dwordx4 v255, s[52:55], s4 offen offset:0 sc1 lds\n"
    "buffer_load_dwordx4 v255, s[52:55], s4 offen offset:1024 sc1 lds\n"
    "buffer_load_dwordx4 v255, s[52:55], s4 offen offset:2048 sc1 lds\n"
    "s_add_u32 s4, s4, 12288\n"
    "s_add_u32 m0, s17, 61440\n"
    "s_nop 0\n"
    "buffer_load_dwordx4 v255, s[52:55], s4 offen offset:0 sc1 lds\n"
    "buffer_load_dwordx4 v255, s[52:55], s4 offen offset:1024 sc1 lds\n"
    "buffer_load_dwordx4 v255, s[52:55], s4 offen offset:2048 sc1 lds\n"
    "s_add_u32 s4, s4, 12288\n"
    "s_waitcnt vmcnt(3)\n"
    "Lprime2%=:\n"
    "s_barrier\n"
    "ds_read_b64_tr_b16 v[144:145], v180 offset:0\n"
    "ds_read_b64_tr_b16 v[146:147], v180 offset:4096\n"
    "ds_read_b64_tr_b16 v[148:149], v181 offset:0\n"
    "ds_read_b64_tr_b16 v[150:151], v181 offset:4096\n"
    "ds_read_b64_tr_b16 v[152:153], v180 offset:256\n"
    "ds_read_b64_tr_b16 v[154:155], v180 offset:4352\n"
    "ds_read_b64_tr_b16 v[156:157], v181 offset:256\n"
    "ds_read_b64_tr_b16 v[158:159], v181 offset:4352\n"
    "ds_read_b128 v[96:99], v178 offset:0\n"
    "ds_read_b128 v[100:103], v178 offset:1024\n"
    "ds_read_b128 v[104:107], v178 offset:2048\n"
    "ds_read_b128 v[108:111], v178 offset:3072\n"
    "ds_read_b128 v[112:115], v178 offset:4096\n"
    "ds_read_b128 v[116:119], v178 offset:5120\n"
    "Lop%=:\n"
    "s_add_u32 s15, s15, 1\n"
    "s_lshr_b32 s16, s71, 8\n"
    "s_add_u32 s16, s16, s15\n"
    "s_and_b32 s16, s16, 3\n"
    "s_and_b32 s83, s15, 12\n"
    "s_add_u32 s16, s16, s83\n"
    "s_lshl_b32 s5, s16, 17\n"
    "s_mul_i32 s4, s16, 49152\n"
    "s_mov_b32 s23, 272\n"
    "s_bitcmp1_b32 s23, s15\n"
    "s_cbranch_scc0 Lg1e%=\n"
    "v_cmp_eq_u64 vcc, s[88:89], v[176:177]\n"
    "s_and_b32 s83, vcc_lo, 0xff\n"
    "s_bitcmp1_b32 s83, s16\n"
    "s_cselect_b32 s54, s22, 0\n"
    "s_branch Lgo1%=\n"
    "Lg1e%=:\n"
    "s_mov_b32 s12, 0x40000\n"
    "Lg1%=:\n"
    "v_cmp_eq_u64 vcc, s[88:89], v[176:177]\n"
    "s_bitcmp1_b32 vcc_lo, s16\n"
    "s_cbranch_scc1 Lgo1%=\n"
    "s_sleep 2\n"
    "buffer_load_dwordx2 v[176:177], v254, s[64:67], s74 offen sc1\n"
    "s_waitcnt vmcnt(0)\n"
    "s_sub_u32 s12, s12, 1\n"
    "s_cmp_lg_u32 s12, 0\n"
    "s_cbranch_scc1 Lg1%=\n"
    "Lgo1%=:\n"
    "s_waitcnt vmcnt(10) lgkmcnt(0)\n"
    "s_barrier\n"
    "v_mfma_f32_16x16x32_bf16 v[0:3], v[144:147], v[96:99], v[0:3]\n"
    "s_add_u32 m0, s18, 0\n"
    "ds_read_b64_tr_b16 v[160:161], v180 offset:20480\n"
    "v_mfma_f32_16x16x32_bf16 v[4:7], v[148:151], v[96:99], v[4:7]\n"
    "buffer_load_dwordx4 v250, s[56:59], s5 offen offset:0  lds\n"
    "v_mfma_f32_16x16x32_bf16 v[8:11], v[152:155], v[96:99], v[8:11]\n"
    "ds_read_b64_tr_b16 v[162:163], v180 offset:24576\n"
    "v_mfma_f32_16x16x32_bf16 v[12:15], v[156:159], v[96:99], v[12:15]\n"
    "ds_read_b64_tr_b16 v[164:165], v181 offset:20480\n"
    "v_mfma_f32_16x16x32_bf16 v[16:19], v[144:147], v[100:103], v[16:19]\n"
    "ds_read_b64_tr_b16 v[166:167], v181 offset:24576\n"
    "v_mfma_f32_16x16x32_bf16 v[20:23], v[148:151], v[100:103], v[20:23]\n"
    "buffer_load_dwordx4 v251, s[56:59], s5 offen offset:1024  lds\n"
    "v_mfma_f32_16x16x32_bf16 v[24:27], v[152:155], v[100:103], v[24:27]\n"
    "ds_read_b64_tr_b16 v[168:169], v180 offset:20736\n"
    "v_mfma_f32_16x16x32_bf16 v[28:31], v[156:159], v[100:103], v[28:31]\n"
    "ds_read_b64_tr_b16 v[170:171], v180 offset:24832\n"
    "v_mfma_f32_16x16x32_bf16 v[32:35], v[144:147], v[104:107], v[32:35]\n"
    "ds_read_b64_tr_b16 v[172:173], v181 offset:20736\n"
    "v_mfma_f32_16x16x32_bf16 v[36:39], v[148:151], v[104:107], v[36:39]\n"
    "s_add_u32 m0, s17, 0\n"
    "s_add_u32 s5, s5, 0x8000\n"
    "v_mfma_f32_16x16x32_bf16 v[40:43], v[152:155], v[104:107], v[40:43]\n"
    "buffer_load_dwordx4 v255, s[52:55], s4 offen offset:0 sc1 lds\n"
    "v_mfma_f32_16x16x32_bf16 v[44:47], v[156:159], v[104:107], v[44:47]\n"
    "ds_read_b64_tr_b16 v[174:175], v181 offset:24832\n"
    "v_mfma_f32_16x16x32_bf16 v[48:51], v[144:147], v[108:111], v[48:51]\n"
    "ds_read_b128 v[120:123], v178 offset:20480\n"
    "v_mfma_f32_16x16x32_bf16 v[52:55], v[148:151], v[108:111], v[52:55]\n"
    "ds_read_b128 v[124:127], v178 offset:21504\n"
    "v_mfma_f32_16x16x32_bf16 v[56:59], v[152:155], v[108:111], v[56:59]\n"
    "buffer_load_dwordx4 v255, s[52:55], s4 offen offset:1024 sc1 lds\n"
    "v_mfma_f32_16x16x32_bf16 v[60:63], v[156:159], v[108:111], v[60:63]\n"
    "ds_read_b128 v[128:131], v178 offset:22528\n"
    "v_mfma_f32_16x16x32_bf16 v[64:67], v[144:147], v[112:115], v[64:67]\n"
    "ds_read_b128 v[132:135], v178 offset:23552\n"
    "v_mfma_f32_16x16x32_bf16 v[68:71], v[148:151], v[112:115], v[68:71]\n"
    "ds_read_b128 v[136:139], v178 offset:24576\n"
    "v_mfma_f32_16x16x32_bf16 v[72:75], v[152:155], v[112:115], v[72:75]\n"
    "ds_read_b128 v[140:143], v178 offset:25600\n"
    "v_mfma_f32_16x16x32_bf16 v[76:79], v[156:159], v[112:115], v[76:79]\n"
    "buffer_load_dwordx4 v255, s[52:55], s4 offen offset:2048 sc1 lds\n"
    "v_mfma_f32_16x16x32_bf16 v[80:83], v[144:147], v[116:119], v[80:83]\n"
    "s_add_u32 s4, s4, 12288\n"
    "v_mfma_f32_16x16x32_bf16 v[84:87], v[148:151], v[116:119], v[84:87]\n"
    "v_mfma_f32_16x16x32_bf16 v[88:91], v[152:155], v[116:119], v[88:91]\n"
    "v_mfma_f32_16x16x32_bf16 v[92:95], v[156:159], v[116:119], v[92:95]\n"
    "s_waitcnt vmcnt(10) lgkmcnt(0)\n"
    "s_barrier\n"
    "v_mfma_f32_16x16x32_bf16 v[0:3], v[160:163], v[120:123], v[0:3]\n"
    "s_add_u32 m0, s18, 20480\n"
    "buffer_load_dwordx2 v[176:177], v254, s[64:67], s74 offen sc1\n"
    "v_mfma_f32_16x16x32_bf16 v[4:7], v[164:167], v[120:123], v[4:7]\n"
    "buffer_load_dwordx4 v250, s[56:59], s5 offen offset:0  lds\n"
    "v_mfma_f32_16x16x32_bf16 v[8:11], v[168:171], v[120:123], v[8:11]\n"
    "ds_read_b64_tr_b16 v[144:145], v182 offset:0\n"
    "v_mfma_f32_16x16x32_bf16 v[12:15], v[172:175], v[120:123], v[12:15]\n"
    "ds_read_b64_tr_b16 v[146:147], v182 offset:4096\n"
    "v_mfma_f32_16x16x32_bf16 v[16:19], v[160:163], v[124:127], v[16:19]\n"
    "ds_read_b64_tr_b16 v[148:149], v183 offset:0\n"
    "v_mfma_f32_16x16x32_bf16 v[20:23], v[164:167], v[124:127], v[20:23]\n"
    "buffer_load_dwordx4 v251, s[56:59], s5 offen offset:1024  lds\n"
    "v_mfma_f32_16x16x32_bf16 v[24:27], v[168:171], v[124:127], v[24:27]\n"
    "ds_read_b64_tr_b16 v[150:151], v183 offset:4096\n"
    "v_mfma_f32_16x16x32_bf16 v[28:31], v[172:175], v[124:127], v[28:31]\n"
    "ds_read_b64_tr_b16 v[152:153], v182 offset:256\n"
    "v_mfma_f32_16x16x32_bf16 v[32:35], v[160:163], v[128:131], v[32:35]\n"
    "ds_read_b64_tr_b16 v[154:155], v182 offset:4352\n"
    "v_mfma_f32_16x16x32_bf16 v[36:39], v[164:167], v[128:131], v[36:39]\n"
    "s_add_u32 m0, s17, 20480\n"
    "s_add_u32 s5, s5, 0x8000\n"
    "v_mfma_f32_16x16x32_bf16 v[40:43], v[168:171], v[128:131], v[40:43]\n"
    "buffer_load_dwordx4 v255, s[52:55], s4 offen offset:0 sc1 lds\n"
    "v_mfma_f32_16x16x32_bf16 v[44:47], v[172:175], v[128:131], v[44:47]\n"
    "ds_read_b64_tr_b16 v[156:157], v183 offset:256\n"
    "v_mfma_f32_16x16x32_bf16 v[48:51], v[160:163], v[132:135], v[48:51]\n"
    "ds_read_b64_tr_b16 v[158:159], v183 offset:4352\n"
    "v_mfma_f32_16x16x32_bf16 v[52:55], v[164:167], v[132:135], v[52:55]\n"
    "ds_read_b128 v[96:99], v179 offset:0\n"
    "v_mfma_f32_16x16x32_bf16 v[56:59], v[168:171], v[132:135], v[56:59]\n"
    "buffer_load_dwordx4 v255, s[52:55], s4 offen offset:1024 sc1 lds\n"
    "v_mfma_f32_16x16x32_bf16 v[60:63], v[172:175], v[132:135], v[60:63]\n"
    "ds_read_b128 v[100:103], v179 offset:1024\n"
    "v_mfma_f32_16x16x32_bf16 v[64:67], v[160:163], v[136:139], v[64:67]\n"
    "ds_read_b128 v[104:107], v179 offset:2048\n"
    "v_mfma_f32_16x16x32_bf16 v[68:71], v[164:167], v[136:139], v[68:71]\n"
    "ds_read_b128 v[108:111], v179 offset:3072\n"
    "v_mfma_f32_16x16x32_bf16 v[72:75], v[168:171], v[136:139], v[72:75]\n"
    "ds_read_b128 v[112:115], v179 offset:4096\n"
    "v_mfma_f32_16x16x32_bf16 v[76:79], v[172:175], v[136:139], v[76:79]\n"
    "buffer_load_dwordx4 v255, s[52:55], s4 offen offset:2048 sc1 lds\n"
    "v_mfma_f32_16x16x32_bf16 v[80:83], v[160:163], v[140:143], v[80:83]\n"
    "s_add_u32 s4, s4, 12288\n"
    "ds_read_b128 v[116:119], v179 offset:5120\n"
    "v_mfma_f32_16x16x32_bf16 v[84:87], v[164:167], v[140:143], v[84:87]\n"
    "v_mfma_f32_16x16x32_bf16 v[88:91], v[168:171], v[140:143], v[88:91]\n"
    "v_mfma_f32_16x16x32_bf16 v[92:95], v[172:175], v[140:143], v[92:95]\n"
    "s_waitcnt vmcnt(10) lgkmcnt(0)\n"
    "s_barrier\n"
    "v_mfma_f32_16x16x32_bf16 v[0:3], v[144:147], v[96:99], v[0:3]\n"
    "s_add_u32 m0, s18, 40960\n"
    "ds_read_b64_tr_b16 v[160:161], v182 offset:20480\n"
    "v_mfma_f32_16x16x32_bf16 v[4:7], v[148:151], v[96:99], v[4:7]\n"
    "buffer_load_dwordx4 v250, s[56:59], s5 offen offset:0  lds\n"
    "v_mfma_f32_16x16x32_bf16 v[8:11], v[152:155], v[96:99], v[8:11]\n"
    "ds_read_b64_tr_b16 v[162:163], v182 offset:24576\n"
    "v_mfma_f32_16x16x32_bf16 v[12:15], v[156:159], v[96:99], v[12:15]\n"
    "ds_read_b64_tr_b16 v[164:165], v183 offset:20480\n"
    "v_mfma_f32_16x16x32_bf16 v[16:19], v[144:147], v[100:103], v[16:19]\n"
    "ds_read_b64_tr_b16 v[166:167], v183 offset:24576\n"
    "v_mfma_f32_16x16x32_bf16 v[20:23], v[148:151], v[100:103], v[20:23]\n"
    "buffer_load_dwordx4 v251, s[56:59], s5 offen offset:1024  lds\n"
    "v_mfma_f32_16x16x32_bf16 v[24:27], v[152:155], v[100:103], v[24:27]\n"
    "ds_read_b64_tr_b16 v[168:169], v182 offset:20736\n"
    "v_mfma_f32_16x16x32_bf16 v[28:31], v[156:159], v[100:103], v[28:31]\n"
    "ds_read_b64_tr_b16 v[170:171], v182 offset:24832\n"
    "v_mfma_f32_16x16x32_bf16 v[32:35], v[144:147], v[104:107], v[32:35]\n"
    "ds_read_b64_tr_b16 v[172:173], v183 offset:20736\n"
    "v_mfma_f32_16x16x32_bf16 v[36:39], v[148:151], v[104:107], v[36:39]\n"
    "s_add_u32 m0, s17, 40960\n"
    "s_add_u32 s5, s5, 0x8000\n"
    "v_mfma_f32_16x16x32_bf16 v[40:43], v[152:155], v[104:107], v[40:43]\n"
    "buffer_load_dwordx4 v255, s[52:55], s4 offen offset:0 sc1 lds\n"
    "v_mfma_f32_16x16x32_bf16 v[44:47], v[156:159], v[104:107], v[44:47]\n"
    "ds_read_b64_tr_b16 v[174:175], v183 offset:24832\n"
    "v_mfma_f32_16x16x32_bf16 v[48:51], v[144:147], v[108:111], v[48:51]\n"
    "ds_read_b128 v[120:123], v179 offset:20480\n"
    "v_mfma_f32_16x16x32_bf16 v[52:55], v[148:151], v[108:111], v[52:55]\n"
    "ds_read_b128 v[124:127], v179 offset:21504\n"
    "v_mfma_f32_16x16x32_bf16 v[56:59], v[152:155], v[108:111], v[56:59]\n"
    "buffer_load_dwordx4 v255, s[52:55], s4 offen offset:1024 sc1 lds\n"
    "v_mfma_f32_16x16x32_bf16 v[60:63], v[156:159], v[108:111], v[60:63]\n"
    "ds_read_b128 v[128:131], v179 offset:22528\n"
    "v_mfma_f32_16x16x32_bf16 v[64:67], v[144:147], v[112:115], v[64:67]\n"
    "ds_read_b128 v[132:135], v179 offset:23552\n"
    "v_mfma_f32_16x16x32_bf16 v[68:71], v[148:151], v[112:115], v[68:71]\n"
    "ds_read_b128 v[136:139], v179 offset:24576\n"
    "v_mfma_f32_16x16x32_bf16 v[72:75], v[152:155], v[112:115], v[72:75]\n"
    "ds_read_b128 v[140:143], v179 offset:25600\n"
    "v_mfma_f32_16x16x32_bf16 v[76:79], v[156:159], v[112:115], v[76:79]\n"
    "buffer_load_dwordx4 v255, s[52:55], s4 offen offset:2048 sc1 lds\n"
    "v_mfma_f32_16x16x32_bf16 v[80:83], v[144:147], v[116:119], v[80:83]\n"
    "s_add_u32 s4, s4, 12288\n"
    "v_mfma_f32_16x16x32_bf16 v[84:87], v[148:151], v[116:119], v[84:87]\n"
    "v_mfma_f32_16x16x32_bf16 v[88:91], v[152:155], v[116:119], v[88:91]\n"
    "v_mfma_f32_16x16x32_bf16 v[92:95], v[156:159], v[116:119], v[92:95]\n"
    "s_waitcnt vmcnt(10) lgkmcnt(0)\n"
    "s_barrier\n"
    "v_mfma_f32_16x16x32_bf16 v[0:3], v[160:163], v[120:123], v[0:3]\n"
    "s_add_u32 m0, s18, 61440\n"
    "ds_read_b64_tr_b16 v[144:145], v180 offset:0\n"
    "v_mfma_f32_16x16x32_bf16 v[4:7], v[164:167], v[120:123], v[4:7]\n"
    "buffer_load_dwordx4 v250, s[56:59], s5 offen offset:0  lds\n"
    "v_mfma_f32_16x16x32_bf16 v[8:11], v[168:171], v[120:123], v[8:11]\n"
    "ds_read_b64_tr_b16 v[146:147], v180 offset:4096\n"
    "v_mfma_f32_16x16x32_bf16 v[12:15], v[172:175], v[120:123], v[12:15]\n"
    "ds_read_b64_tr_b16 v[148:149], v181 offset:0\n"
    "v_mfma_f32_16x16x32_bf16 v[16:19], v[160:163], v[124:127], v[16:19]\n"
    "ds_read_b64_tr_b16 v[150:151], v181 offset:4096\n"
    "v_mfma_f32_16x16x32_bf16 v[20:23], v[164:167], v[124:127], v[20:23]\n"
    "buffer_load_dwordx4 v251, s[56:59], s5 offen offset:1024  lds\n"
    "v_mfma_f32_16x16x32_bf16 v[24:27], v[168:171], v[124:127], v[24:27]\n"
    "ds_read_b64_tr_b16 v[152:153], v180 offset:256\n"
    "v_mfma_f32_16x16x32_bf16 v[28:31], v[172:175], v[124:127], v[28:31]\n"
    "ds_read_b64_tr_b16 v[154:155], v180 offset:4352\n"
    "v_mfma_f32_16x16x32_bf16 v[32:35], v[160:163], v[128:131], v[32:35]\n"
    "ds_read_b64_tr_b16 v[156:157], v181 offset:256\n"
    "v_mfma_f32_16x16x32_bf16 v[36:39], v[164:167], v[128:131], v[36:39]\n"
    "s_add_u32 m0, s17, 61440\n"
    "s_add_u32 s5, s5, 0x8000\n"
    "v_mfma_f32_16x16x32_bf16 v[40:43], v[168:171], v[128:131], v[40:43]\n"
    "buffer_load_dwordx4 v255, s[52:55], s4 offen offset:0 sc1 lds\n"
    "v_mfma_f32_16x16x32_bf16 v[44:47], v[172:175], v[128:131], v[44:47]\n"
    "ds_read_b64_tr_b16 v[158:159], v181 offset:4352\n"
    "v_mfma_f32_16x16x32_bf16 v[48:51], v[160:163], v[132:135], v[48:51]\n"
    "ds_read_b128 v[96:99], v178 offset:0\n"
    "v_mfma_f32_16x16x32_bf16 v[52:55], v[164:167], v[132:135], v[52:55]\n"
    "ds_read_b128 v[100:103], v178 offset:1024\n"
    "v_mfma_f32_16x16x32_bf16 v[56:59], v[168:171], v[132:135], v[56:59]\n"
    "buffer_load_dwordx4 v255, s[52:55], s4 offen offset:1024 sc1 lds\n"
    "v_mfma_f32_16x16x32_bf16 v[60:63], v[172:175], v[132:135], v[60:63]\n"
    "ds_read_b128 v[104:107], v178 offset:2048\n"
    "v_mfma_f32_16x16x32_bf16 v[64:67], v[160:163], v[136:139], v[64:67]\n"
    "ds_read_b128 v[108:111], v178 offset:3072\n"
    "v_mfma_f32_16x16x32_bf16 v[68:71], v[164:167], v[136:139], v[68:71]\n"
    "ds_read_b128 v[112:115], v178 offset:4096\n"
    "v_mfma_f32_16x16x32_bf16 v[72:75], v[168:171], v[136:139], v[72:75]\n"
    "ds_read_b128 v[116:119], v178 offset:5120\n"
    "v_mfma_f32_16x16x32_bf16 v[76:79], v[172:175], v[136:139], v[76:79]\n"
    "buffer_load_dwordx4 v255, s[52:55], s4 offen offset:2048 sc1 lds\n"
    "v_mfma_f32_16x16x32_bf16 v[80:83], v[160:163], v[140:143], v[80:83]\n"
    "s_add_u32 s4, s4, 12288\n"
    "v_mfma_f32_16x16x32_bf16 v[84:87], v[164:167], v[140:143], v[84:87]\n"
    "v_mfma_f32_16x16x32_bf16 v[88:91], v[168:171], v[140:143], v[88:91]\n"
    "v_mfma_f32_16x16x32_bf16 v[92:95], v[172:175], v[140:143], v[92:95]\n"
    "s_bitcmp1_b32 s23, s15\n"
    "s_cbranch_scc0 Lop%=\n"
    "s_cmp_eq_u32 s15, 8\n"
    "s_cbranch_scc1 Lodone%=\n"
    "s_waitcnt lgkmcnt(0)\n"
    "s_cmp_eq_u32 s54, 0\n"
    "s_cbranch_scc1 Lprime%=\n"
    "s_branch Lprime2%=\n"
    "Lodone%=:\n"
    "s_waitcnt vmcnt(0) lgkmcnt(0)\n"
    "s_barrier\n"
    "v_mov_b32 v178, s88\n"
    "v_mov_b32 v179, s89\n"
    "s_lshr_b32 s33, s20, 12\n"
    "s_cmp_eq_u32 s33, s21\n"
    "s_cbranch_scc1 Lkeepq%=\n"
    "s_lshl_b32 s34, s33, 2\n"
    "s_add_u32 s34, s34, s21\n"
    "s_mul_i32 s35, s34, 24576\n"
    "v_cvt_pk_bf16_f32 v96, v0, v1\n"
    "v_cvt_pk_bf16_f32 v97, v2, v3\n"
    "v_cvt_pk_bf16_f32 v98, v4, v5\n"
    "v_cvt_pk_bf16_f32 v99, v6, v7\n"
    "buffer_store_dwordx4 v[96:99], v255, s[44:47], s35 offen offset:0 sc1\n"
    "v_cvt_pk_bf16_f32 v100, v8, v9\n"
    "v_cvt_pk_bf16_f32 v101, v10, v11\n"
    "v_cvt_pk_bf16_f32 v102, v12, v13\n"
    "v_cvt_pk_bf16_f32 v103, v14, v15\n"
    "buffer_store_dwordx4 v[100:103], v255, s[44:47], s35 offen offset:1024 sc1\n"
    "v_cvt_pk_bf16_f32 v104, v16, v17\n"
    "v_cvt_pk_bf16_f32 v105, v18, v19\n"
    "v_cvt_pk_bf16_f32 v106, v20, v21\n"
    "v_cvt_pk_bf16_f32 v107, v22, v23\n"
    "buffer_store_dwordx4 v[104:107], v255, s[44:47], s35 offen offset:2048 sc1\n"
    "v_cvt_pk_bf16_f32 v108, v24, v25\n"
    "v_cvt_pk_bf16_f32 v109, v26, v27\n"
    "v_cvt_pk_bf16_f32 v110, v28, v29\n"
    "v_cvt_pk_bf16_f32 v111, v30, v31\n"
    "buffer_store_dwordx4 v[108:111], v255, s[44:47], s35 offen offset:3072 sc1\n"
    "s_add_u32 s35, s35, 4096\n"
    "v_cvt_pk_bf16_f32 v112, v32, v33\n"
    "v_cvt_pk_bf16_f32 v113, v34, v35\n"
    "v_cvt_pk_bf16_f32 v114, v36, v37\n"
    "v_cvt_pk_bf16_f32 v115, v38, v39\n"
    "buffer_store_dwordx4 v[112:115], v255, s[44:47], s35 offen offset:0 sc1\n"
    "v_cvt_pk_bf16_f32 v116, v40, v41\n"
    "v_cvt_pk_bf16_f32 v117, v42, v43\n"
    "v_cvt_pk_bf16_f32 v118, v44, v45\n"
    "v_cvt_pk_bf16_f32 v119, v46, v47\n"
    "buffer_store_dwordx4 v[116:119], v255, s[44:47], s35 offen offset:1024 sc1\n"
    "v_cvt_pk_bf16_f32 v120, v48, v49\n"
    "v_cvt_pk_bf16_f32 v121, v50, v51\n"
    "v_cvt_pk_bf16_f32 v122, v52, v53\n"
    "v_cvt_pk_bf16_f32 v123, v54, v55\n"
    "buffer_store_dwordx4 v[120:123], v255, s[44:47], s35 offen offset:2048 sc1\n"
    "v_cvt_pk_bf16_f32 v124, v56, v57\n"
    "v_cvt_pk_bf16_f32 v125, v58, v59\n"
    "v_cvt_pk_bf16_f32 v126, v60, v61\n"
    "v_cvt_pk_bf16_f32 v127, v62, v63\n"
    "buffer_store_dwordx4 v[124:127], v255, s[44:47], s35 offen offset:3072 sc1\n"
    "s_add_u32 s35, s35, 4096\n"
    "v_cvt_pk_bf16_f32 v128, v64, v65\n"
    "v_cvt_pk_bf16_f32 v129, v66, v67\n"
    "v_cvt_pk_bf16_f32 v130, v68, v69\n"
    "v_cvt_pk_bf16_f32 v131, v70, v71\n"
    "buffer_store_dwordx4 v[128:131], v255, s[44:47], s35 offen offset:0 sc1\n"
    "v_cvt_pk_bf16_f32 v132, v72, v73\n"
    "v_cvt_pk_bf16_f32 v133, v74, v75\n"
    "v_cvt_pk_bf16_f32 v134, v76, v77\n"
    "v_cvt_pk_bf16_f32 v135, v78, v79\n"
    "buffer_store_dwordx4 v[132:135], v255, s[44:47], s35 offen offset:1024 sc1\n"
    "v_cvt_pk_bf16_f32 v136, v80, v81\n"
    "v_cvt_pk_bf16_f32 v137, v82, v83\n"
    "v_cvt_pk_bf16_f32 v138, v84, v85\n"
    "v_cvt_pk_bf16_f32 v139, v86, v87\n"
    "buffer_store_dwordx4 v[136:139], v255, s[44:47], s35 offen offset:2048 sc1\n"
    "v_cvt_pk_bf16_f32 v140, v88, v89\n"
    "v_cvt_pk_bf16_f32 v141, v90, v91\n"
    "v_cvt_pk_bf16_f32 v142, v92, v93\n"
    "v_cvt_pk_bf16_f32 v143, v94, v95\n"
    "buffer_store_dwordx4 v[140:143], v255, s[44:47], s35 offen offset:3072 sc1\n"
    "s_add_u32 s35, s35, 4096\n"
    "s_lshl_b32 s34, s34, 3\n"
    "s_add_u32 s34, s34, s69\n"
    "s_waitcnt vmcnt(0)\n"
    "s_mov_b64 exec, 1\n"
    "buffer_store_dwordx2 v[178:179], v255, s[64:67], s34 offen sc1\n"
    "s_mov_b64 exec, -1\n"
    "s_branch Lxw%=\n"
    "Lkeepq%=:\n"
    "v_add_u32 v178, s19, v255\n"
    "ds_write_b128 v178, v[0:3] offset:0\n"
    "ds_write_b128 v178, v[4:7] offset:1024\n"
    "ds_write_b128 v178, v[8:11] offset:2048\n"
    "ds_write_b128 v178, v[12:15] offset:3072\n"
    "ds_write_b128 v178, v[16:19] offset:4096\n"
    "ds_write_b128 v178, v[20:23] offset:5120\n"
    "ds_write_b128 v178, v[24:27] offset:6144\n"
    "ds_write_b128 v178, v[28:31] offset:7168\n"
    "ds_write_b128 v178, v[32:35] offset:8192\n"
    "ds_write_b128 v178, v[36:39] offset:9216\n"
    "ds_write_b128 v178, v[40:43] offset:10240\n"
    "ds_write_b128 v178, v[44:47] offset:11264\n"
    "ds_write_b128 v178, v[48:51] offset:12288\n"
    "ds_write_b128 v178, v[52:55] offset:13312\n"
    "ds_write_b128 v178, v[56:59] offset:14336\n"
    "ds_write_b128 v178, v[60:63] offset:15360\n"
    "ds_write_b128 v178, v[64:67] offset:16384\n"
    "ds_write_b128 v178, v[68:71] offset:17408\n"
    "ds_write_b128 v178, v[72:75] offset:18432\n"
    "ds_write_b128 v178, v[76:79] offset:19456\n"
    "ds_write_b128 v178, v[80:83] offset:20480\n"
    "ds_write_b128 v178, v[84:87] offset:21504\n"
    "ds_write_b128 v178, v[88:91] offset:22528\n"
    "ds_write_b128 v178, v[92:95] offset:23552\n"
    "Lxw%=:\n"
    "s_waitcnt lgkmcnt(0)\n"
    "s_barrier\n"
    "s_lshl_b32 s34, s21, 5\n"
    "s_add_u32 s34, s34, s69\n"
    "s_lshl_b32 s35, 1, s21\n"
    "s_mul_i32 s83, s33, 6144\n"
    "s_add_u32 s4, s83, s19\n"
    "v_add_u32 v178, s4, v255\n"
    "ds_read_b128 v[0:3], v178 offset:0\n"
    "ds_read_b128 v[4:7], v178 offset:1024\n"
    "ds_read_b128 v[8:11], v178 offset:2048\n"
    "ds_read_b128 v[12:15], v178 offset:3072\n"
    "ds_read_b128 v[16:19], v178 offset:4096\n"
    "ds_read_b128 v[20:23], v178 offset:5120\n"
    "s_mov_b32 s12, 0x40000\n"
    "Lxp%=:\n"
    "buffer_load_dwordx2 v[176:177], v254, s[64:67], s34 offen sc1\n"
    "s_waitcnt vmcnt(0)\n"
    "v_cmp_eq_u64 vcc, s[88:89], v[176:177]\n"
    "s_or_b32 s5, vcc_lo, s35\n"
    "s_and_b32 s5, s5, 15\n"
    "s_cmp_eq_u32 s5, 15\n"
    "s_cbranch_scc1 Lxg%=\n"
    "s_sleep 2\n"
    "s_sub_u32 s12, s12, 1\n"
    "s_cmp_lg_u32 s12, 0\n"
    "s_cbranch_scc1 Lxp%=\n"
    "Lxg%=:\n"
    "s_add_u32 s5, s21, 1\n"
    "s_and_b32 s5, s5, 3\n"
    "s_lshl_b32 s15, s21, 2\n"
    "s_add_u32 s5, s5, s15\n"
    "s_mul_i32 s5, s5, 24576\n"
    "s_lshr_b32 s15, s83, 1\n"
    "s_add_u32 s5, s5, s15\n"
    "buffer_load_dwordx4 v[24:27], v255, s[44:47], s5 offen offset:0 sc1\n"
    "buffer_load_dwordx4 v[28:31], v255, s[44:47], s5 offen offset:1024 sc1\n"
    "buffer_load_dwordx4 v[32:35], v255, s[44:47], s5 offen offset:2048 sc1\n"
    "s_add_u32 s5, s21, 2\n"
    "s_and_b32 s5, s5, 3\n"
    "s_lshl_b32 s15, s21, 2\n"
    "s_add_u32 s5, s5, s15\n"
    "s_mul_i32 s5, s5, 24576\n"
    "s_lshr_b32 s15, s83, 1\n"
    "s_add_u32 s5, s5, s15\n"
    "buffer_load_dwordx4 v[48:51], v255, s[44:47], s5 offen offset:0 sc1\n"
    "buffer_load_dwordx4 v[52:55], v255, s[44:47], s5 offen offset:1024 sc1\n"
    "buffer_load_dwordx4 v[56:59], v255, s[44:47], s5 offen offset:2048 sc1\n"
    "s_add_u32 s5, s21, 3\n"
    "s_and_b32 s5, s5, 3\n"
    "s_lshl_b32 s15, s21, 2\n"
    "s_add_u32 s5, s5, s15\n"
    "s_mul_i32 s5, s5, 24576\n"
    "s_lshr_b32 s15, s83, 1\n"
    "s_add_u32 s5, s5, s15\n"
    "buffer_load_dwordx4 v[72:75], v255, s[44:47], s5 offen offset:0 sc1\n"
    "buffer_load_dwordx4 v[76:79], v255, s[44:47], s5 offen offset:1024 sc1\n"
    "buffer_load_dwordx4 v[80:83], v255, s[44:47], s5 offen offset:2048 sc1\n"
    "v_bfe_u32 v179, v255, 4, 4\n"
    "v_lshlrev_b32 v179, 11, v179\n"
    "v_lshrrev_b32 v180, 8, v255\n"
    "v_lshl_add_u32 v179, v180, 4, v179\n"
    "s_waitcnt vmcnt(6)\n"
    "v_lshlrev_b32 v36, 16, v24\n"
    "v_and_b32 v37, 0xffff0000, v24\n"
    "s_waitcnt lgkmcnt(5)\n"
    "v_add_f32 v0, v0, v36\n"
    "v_add_f32 v1, v1, v37\n"
    "v_lshlrev_b32 v38, 16, v25\n"
    "v_and_b32 v39, 0xffff0000, v25\n"
    "v_add_f32 v2, v2, v38\n"
    "v_add_f32 v3, v3, v39\n"
    "v_lshlrev_b32 v40, 16, v26\n"
    "v_and_b32 v41, 0xffff0000, v26\n"
    "s_waitcnt lgkmcnt(4)\n"
    "v_add_f32 v4, v4, v40\n"
    "v_add_f32 v5, v5, v41\n"
    "v_lshlrev_b32 v42, 16, v27\n"
    "v_and_b32 v43, 0xffff0000, v27\n"
    "v_add_f32 v6, v6, v42\n"
    "v_add_f32 v7, v7, v43\n"
    "v_lshlrev_b32 v36, 16, v28\n"
    "v_and_b32 v37, 0xffff0000, v28\n"
    "s_waitcnt lgkmcnt(3)\n"
    "v_add_f32 v8, v8, v36\n"
    "v_add_f32 v9, v9, v37\n"
    "v_lshlrev_b32 v38, 16, v29\n"
    "v_and_b32 v39, 0xffff0000, v29\n"
    "v_add_f32 v10, v10, v38\n"
    "v_add_f32 v11, v11, v39\n"
    "v_lshlrev_b32 v40, 16, v30\n"
    "v_and_b32 v41, 0xffff0000, v30\n"
    "s_waitcnt lgkmcnt(2)\n"
    "v_add_f32 v12, v12, v40\n"
    "v_add_f32 v13, v13, v41\n"
    "v_lshlrev_b32 v42, 16, v31\n"
    "v_and_b32 v43, 0xffff0000, v31\n"
    "v_add_f32 v14, v14, v42\n"
    "v_add_f32 v15, v15, v43\n"
    "v_lshlrev_b32 v36, 16, v32\n"
    "v_and_b32 v37, 0xffff0000, v32\n"
    "s_waitcnt lgkmcnt(1)\n"
    "v_add_f32 v16, v16, v36\n"
    "v_add_f32 v17, v17, v37\n"
    "v_lshlrev_b32 v38, 16, v33\n"
    "v_and_b32 v39, 0xffff0000, v33\n"
    "v_add_f32 v18, v18, v38\n"
    "v_add_f32 v19, v19, v39\n"
    "v_lshlrev_b32 v40, 16, v34\n"
    "v_and_b32 v41, 0xffff0000, v34\n"
    "s_waitcnt lgkmcnt(0)\n"
    "v_add_f32 v20, v20, v40\n"
    "v_add_f32 v21, v21, v41\n"
    "v_lshlrev_b32 v42, 16, v35\n"
    "v_and_b32 v43, 0xffff0000, v35\n"
    "v_add_f32 v22, v22, v42\n"
    "v_add_f32 v23, v23, v43\n"
    "s_waitcnt vmcnt(3)\n"
    "v_lshlrev_b32 v60, 16, v48\n"
    "v_and_b32 v61, 0xffff0000, v48\n"
    "v_add_f32 v0, v0, v60\n"
    "v_add_f32 v1, v1, v61\n"
    "v_lshlrev_b32 v62, 16, v49\n"
    "v_and_b32 v63, 0xffff0000, v49\n"
    "v_add_f32 v2, v2, v62\n"
    "v_add_f32 v3, v3, v63\n"
    "v_lshlrev_b32 v64, 16, v50\n"
    "v_and_b32 v65, 0xffff0000, v50\n"
    "v_add_f32 v4, v4, v64\n"
    "v_add_f32 v5, v5, v65\n"
    "v_lshlrev_b32 v66, 16, v51\n"
    "v_and_b32 v67, 0xffff0000, v51\n"
    "v_add_f32 v6, v6, v66\n"
    "v_add_f32 v7, v7, v67\n"
    "v_lshlrev_b32 v60, 16, v52\n"
    "v_and_b32 v61, 0xffff0000, v52\n"
    "v_add_f32 v8, v8, v60\n"
    "v_add_f32 v9, v9, v61\n"
    "v_lshlrev_b32 v62, 16, v53\n"
    "v_and_b32 v63, 0xffff0000, v53\n"
    "v_add_f32 v10, v10, v62\n"
    "v_add_f32 v11, v11, v63\n"
    "v_lshlrev_b32 v64, 16, v54\n"
    "v_and_b32 v65, 0xffff0000, v54\n"
    "v_add_f32 v12, v12, v64\n"
    "v_add_f32 v13, v13, v65\n"
    "v_lshlrev_b32 v66, 16, v55\n"
    "v_and_b32 v67, 0xffff0000, v55\n"
    "v_add_f32 v14, v14, v66\n"
    "v_add_f32 v15, v15, v67\n"
    "v_lshlrev_b32 v60, 16, v56\n"
    "v_and_b32 v61, 0xffff0000, v56\n"
    "v_add_f32 v16, v16, v60\n"
    "v_add_f32 v17, v17, v61\n"
    "v_lshlrev_b32 v62, 16, v57\n"
    "v_and_b32 v63, 0xffff0000, v57\n"
    "v_add_f32 v18, v18, v62\n"
    "v_add_f32 v19, v19, v63\n"
    "v_lshlrev_b32 v64, 16, v58\n"
    "v_and_b32 v65, 0xffff0000, v58\n"
    "v_add_f32 v20, v20, v64\n"
    "v_add_f32 v21, v21, v65\n"
    "v_lshlrev_b32 v66, 16, v59\n"
    "v_and_b32 v67, 0xffff0000, v59\n"
    "v_add_f32 v22, v22, v66\n"
    "v_add_f32 v23, v23, v67\n"
    "s_waitcnt vmcnt(0)\n"
    "v_lshlrev_b32 v84, 16, v72\n"
    "v_and_b32 v85, 0xffff0000, v72\n"
    "v_add_f32 v0, v0, v84\n"
    "v_add_f32 v1, v1, v85\n"
    "v_lshlrev_b32 v86, 16, v73\n"
    "v_and_b32 v87, 0xffff0000, v73\n"
    "v_add_f32 v2, v2, v86\n"
    "v_add_f32 v3, v3, v87\n"
    "v_lshlrev_b32 v88, 16, v74\n"
    "v_and_b32 v89, 0xffff0000, v74\n"
    "v_add_f32 v4, v4, v88\n"
    "v_add_f32 v5, v5, v89\n"
    "v_lshlrev_b32 v90, 16, v75\n"
    "v_and_b32 v91, 0xffff0000, v75\n"
    "v_add_f32 v6, v6, v90\n"
    "v_add_f32 v7, v7, v91\n"
    "v_lshlrev_b32 v84, 16, v76\n"
    "v_and_b32 v85, 0xffff0000, v76\n"
    "v_add_f32 v8, v8, v84\n"
    "v_add_f32 v9, v9, v85\n"
    "v_lshlrev_b32 v86, 16, v77\n"
    "v_and_b32 v87, 0xffff0000, v77\n"
    "v_add_f32 v10, v10, v86\n"
    "v_add_f32 v11, v11, v87\n"
    "v_lshlrev_b32 v88, 16, v78\n"
    "v_and_b32 v89, 0xffff0000, v78\n"
    "v_add_f32 v12, v12, v88\n"
    "v_add_f32 v13, v13, v89\n"
    "v_lshlrev_b32 v90, 16, v79\n"
    "v_and_b32 v91, 0xffff0000, v79\n"
    "v_add_f32 v14, v14, v90\n"
    "v_add_f32 v15, v15, v91\n"
    "v_lshlrev_b32 v84, 16, v80\n"
    "v_and_b32 v85, 0xffff0000, v80\n"
    "v_add_f32 v16, v16, v84\n"
    "v_add_f32 v17, v17, v85\n"
    "v_lshlrev_b32 v86, 16, v81\n"
    "v_and_b32 v87, 0xffff0000, v81\n"
    "v_add_f32 v18, v18, v86\n"
    "v_add_f32 v19, v19, v87\n"
    "v_lshlrev_b32 v88, 16, v82\n"
    "v_and_b32 v89, 0xffff0000, v82\n"
    "v_add_f32 v20, v20, v88\n"
    "v_add_f32 v21, v21, v89\n"
    "v_lshlrev_b32 v90, 16, v83\n"
    "v_and_b32 v91, 0xffff0000, v83\n"
    "v_add_f32 v22, v22, v90\n"
    "v_add_f32 v23, v23, v91\n"
    "v_cvt_pk_bf16_f32 v96, v0, v1\n"
    "v_cvt_pk_bf16_f32 v97, v2, v3\n"
    "v_cvt_pk_bf16_f32 v98, v4, v5\n"
    "v_cvt_pk_bf16_f32 v99, v6, v7\n"
    "v_cndmask_b32_e64 v100, v96, v98, s[84:85]\n"
    "v_cndmask_b32_e64 v101, v97, v99, s[84:85]\n"
    "v_cndmask_b32_e64 v102, v98, v96, s[84:85]\n"
    "v_cndmask_b32_e64 v103, v99, v97, s[84:85]\n"
    "s_mul_i32 s34, s33, 3\n"
    "s_add_u32 s34, s34, 0\n"
    "s_lshr_b32 s35, s34, 1\n"
    "s_lshl_b32 s35, s35, 15\n"
    "s_and_b32 s34, s34, 1\n"
    "s_lshl_b32 s34, s34, 6\n"
    "s_add_u32 s35, s35, s34\n"
    "buffer_store_dwordx4 v[100:103], v179, s[60:63], s35 offen offset:0 nt\n"
    "v_cvt_pk_bf16_f32 v104, v8, v9\n"
    "v_cvt_pk_bf16_f32 v105, v10, v11\n"
    "v_cvt_pk_bf16_f32 v106, v12, v13\n"
    "v_cvt_pk_bf16_f32 v107, v14, v15\n"
    "v_cndmask_b32_e64 v108, v104, v106, s[84:85]\n"
    "v_cndmask_b32_e64 v109, v105, v107, s[84:85]\n"
    "v_cndmask_b32_e64 v110, v106, v104, s[84:85]\n"
    "v_cndmask_b32_e64 v111, v107, v105, s[84:85]\n"
    "s_mul_i32 s34, s33, 3\n"
    "s_add_u32 s34, s34, 1\n"
    "s_lshr_b32 s35, s34, 1\n"
    "s_lshl_b32 s35, s35, 15\n"
    "s_and_b32 s34, s34, 1\n"
    "s_lshl_b32 s34, s34, 6\n"
    "s_add_u32 s35, s35, s34\n"
    "buffer_store_dwordx4 v[108:111], v179, s[60:63], s35 offen offset:0 nt\n"
    "v_cvt_pk_bf16_f32 v112, v16, v17\n"
    "v_cvt_pk_bf16_f32 v113, v18, v19\n"
    "v_cvt_pk_bf16_f32 v114, v20, v21\n"
    "v_cvt_pk_bf16_f32 v115, v22, v23\n"
    "v_cndmask_b32_e64 v116, v112, v114, s[84:85]\n"
    "v_cndmask_b32_e64 v117, v113, v115, s[84:85]\n"
    "v_cndmask_b32_e64 v118, v114, v112, s[84:85]\n"
    "v_cndmask_b32_e64 v119, v115, v113, s[84:85]\n"
    "s_mul_i32 s34, s33, 3\n"
    "s_add_u32 s34, s34, 2\n"
    "s_lshr_b32 s35, s34, 1\n"
    "s_lshl_b32 s35, s35, 15\n"
    "s_and_b32 s34, s34, 1\n"
    "s_lshl_b32 s34, s34, 6\n"
    "s_add_u32 s35, s35, s34\n"
    "buffer_store_dwordx4 v[116:119], v179, s[60:63], s35 offen offset:0 nt\n"
    "Lend%=:\n"
    "s_waitcnt vmcnt(0)\n"
    : "+{v243}"(tra0), "+{v245}"(kra), "+{v250}"(wqo), "+{v254}"(xo), "+{s[68:75]}"(sc), "+{s[52:55]}"(srdOL), "+{s[44:47]}"(srdX)
    : "{v246}"(kvo0), "{v247}"(kvo1), "{v255}"(ooff),
      "{s[36:39]}"(srdKV), "{s[40:43]}"(srdW), "{s[48:51]}"(srdO),
      "{s[56:59]}"(srdWO), "{s[60:63]}"(srdOUT), "{s[64:67]}"(srdFL), "{s99}"(dbgoff), "{s3}"(horder), "{s[100:101]}"(kbase)
    : "v0", "v1", "v2", "v3", "v4", "v5", "v6", "v7", "v8", "v9", "v10", "v11", "v12", "v13", "v14", "v15", "v16", "v17", "v18", "v19", "v20", "v21", "v22", "v23", "v24", "v25", "v26", "v27", "v28", "v29", "v30", "v31", "v32", "v33", "v34", "v35", "v36", "v37", "v38", "v39", "v40", "v41", "v42", "v43", "v44", "v45", "v46", "v47", "v48", "v49", "v50", "v51", "v52", "v53", "v54", "v55", "v56", "v57", "v58", "v59", "v60", "v61", "v62", "v63", "v64", "v65", "v66", "v67", "v68", "v69", "v70", "v71", "v72", "v73", "v74", "v75", "v76", "v77", "v78", "v79", "v80", "v81", "v82", "v83", "v84", "v85", "v86", "v87", "v88", "v89", "v90", "v91", "v92", "v93", "v94", "v95", "v96", "v97", "v98", "v99", "v100", "v101", "v102", "v103", "v104", "v105", "v106", "v107", "v108", "v109", "v110", "v111", "v112", "v113", "v114", "v115", "v116", "v117", "v118", "v119", "v120", "v121", "v122", "v123", "v124", "v125", "v126", "v127", "v128", "v129", "v130", "v131", "v132", "v133", "v134", "v135", "v136", "v137", "v138", "v139", "v140", "v141", "v142", "v143", "v144", "v145", "v146", "v147", "v148", "v149", "v150", "v151", "v152", "v153", "v154", "v155", "v156", "v157", "v158", "v159", "v160", "v161", "v162", "v163", "v164", "v165", "v166", "v167", "v168", "v169", "v170", "v171", "v172", "v173", "v174", "v175", "v176", "v177", "v178", "v179", "v180", "v181", "v182", "v183", "v184", "v185", "v186", "v187", "v188", "v189", "v190", "v191", "v192", "v193", "v194", "v195", "v196", "v197", "v198", "v199", "v200", "v201", "v202", "v203", "v204", "v205", "v206", "v207", "v208", "v209", "v210", "v211", "v212", "v213", "v214", "v215", "v216", "v217", "v218", "v219", "v220", "v221", "v222", "v223", "v224", "v225", "v226", "v227", "v228", "v229", "v230", "v231", "v232", "v233", "v234", "v235", "v236", "v237", "v238", "v239", "v240", "v241", "v242", "v244", "v248", "v249", "v251", "v252", "v253", "s4", "s5", "s6", "s7", "s8", "s9", "s10", "s11", "s12", "s13", "s14", "s15", "s16", "s17", "s18", "s19", "s20", "s21", "s22", "s23", "s24", "s25", "s26", "s27", "s28", "s29", "s30", "s31", "s32", "s33", "s34", "s35", "s76", "s77", "s78", "s79", "s80", "s81", "s82", "s83", "s84", "s85", "s86", "s87", "s88", "s89", "s90", "s91", "s92", "s93", "s94", "s95", "s96", "s97", "s98", "vcc", "scc", "memory");
}

extern "C" void kernel_launch(void* const* d_in, const int* in_sizes, int n_in, void* d_out, int out_size, void* d_ws, size_t ws_size, hipStream_t stream) {
  (void)hipFuncSetAttribute((const void*)fused_kernel, hipFuncAttributeMaxDynamicSharedMemorySize, LDS1);
  fused_kernel<<<512, 256, LDS1, stream>>>((const uint16_t*)d_in[0], (const uint16_t*)d_in[1], (const uint16_t*)d_in[2], (const uint16_t*)d_in[3], (const uint16_t*)d_in[4],
                                             (const uint16_t*)d_in[5], (const uint16_t*)d_in[6], (const uint16_t*)d_in[7], (const uint16_t*)d_in[8], (const uint16_t*)d_in[9],
                                             (uint16_t*)d_out, (char*)d_ws);
}
```
